```python
import torch

def _softmax(x, axis=-1):
    return torch.softmax(x, dim=axis)
def _gelu(x):
    return torch.nn.functional.gelu(x, approximate="tanh")
def _silu(x):
    return torch.nn.functional.silu(x)
def _sigmoid(x):
    return torch.sigmoid(x)
def _layernorm(x, axis=-1, eps=1e-5):
    m = x.mean(dim=axis, keepdim=True)
    v = x.var(dim=axis, keepdim=True, unbiased=False)
    return (x - m) / torch.sqrt(v + eps)
def _scatter_add(tgt, idx, src, axis=0):
    return tgt.clone().index_add_(axis, idx, src)
def _segment_sum(src, seg, n):
    out = torch.zeros((n,) + src.shape[1:], dtype=src.dtype)
    return out.index_add_(0, seg, src)
def _pad(x, pw):
    flat = tuple(v for pair in reversed(pw) for v in pair)
    return torch.nn.functional.pad(x, flat)

kernel_name = 'standalone__deep_cross_wide_deep__recsys'

def setup_inputs():

    _w = (lambda a: (lambda t: t.to(torch.bfloat16) if t.is_floating_point() else t)(torch.as_tensor(a)))
    B, F, D = 4096, 100, 64
    V, Nd = min(100000, 100000), 13
    Din = F * D
    H = 4 * D
    return {
        "idx": _w((lambda lo, hi, *s: torch.randint(lo, hi, s))(0, V, B, F).to(torch.int64)),
        "table": _w((lambda *s: torch.randn(*s, dtype=torch.bfloat16) * 1.6)(V, D) * 0.05),
        "x_wide": _w((lambda *s: torch.randn(*s, dtype=torch.bfloat16) * 1.6)(B, Nd)),
        "w_wide": _w((lambda *s: torch.randn(*s, dtype=torch.bfloat16) * 1.6)(Nd) * 0.05),
        "b_wide": _w((lambda *s: torch.randn(*s, dtype=torch.bfloat16) * 1.6)(1) * 0.01),
        "W1": _w((lambda *s: torch.randn(*s, dtype=torch.bfloat16) * 1.6)(Din, H) * (2.0 / Din) ** 0.5),
        "b1": _w((lambda *s: torch.randn(*s, dtype=torch.bfloat16) * 1.6)(H) * 0.01),
        "W2": _w((lambda *s: torch.randn(*s, dtype=torch.bfloat16) * 1.6)(H) * (1.0 / H) ** 0.5),
        "b2": _w((lambda *s: torch.randn(*s, dtype=torch.bfloat16) * 1.6)(1) * 0.01),
    }

def reference(idx, table, x_wide, w_wide, b_wide, W1, b1, W2, b2):
    emb = table[idx]
    deep_in = emb.reshape(emb.shape[0], -1)
    h = deep_in @ W1 + b1
    h = h * (lambda x, dt: x.to(dt))(h > 0, h.dtype)
    deep_logit = h @ W2 + b2
    wide_logit = x_wide @ w_wide + b_wide
    return _sigmoid(wide_logit + deep_logit)
```

```cpp
#include <hip/hip_runtime.h>
#include <hip/hip_bf16.h>
#include <stdint.h>

typedef __attribute__((ext_vector_type(8))) short bf16x8;
typedef __attribute__((ext_vector_type(4))) short bf16x4;
typedef __attribute__((ext_vector_type(4))) float f32x4;
typedef __attribute__((address_space(3))) void* lds_ptr_t;

constexpr int kB = 4096, kF = 100, kD = 64, kV = 100000, kNd = 13, kH = 256;

#define CFG_BM 64
#define CFG_BN 64
#define CFG_S 1
#define CFG_NST 8
#define CFG_WM 2
#define CFG_WN 2
#define CFG_KXCD 1
#define ABL_NOMFMA 0
#define ABL_NOA 0
#define ABL_NOB 0

__device__ __forceinline__ float bf2f(unsigned short u) { return __uint_as_float(((unsigned)u) << 16); }
__device__ __forceinline__ float rbf(float x) { return __bfloat162float(__float2bfloat16(x)); }

typedef __attribute__((ext_vector_type(4))) int i32x4;
__device__ __forceinline__ i32x4 mk_rsrc(const void* p, unsigned bytes) {
  unsigned long long a = (unsigned long long)p;
  i32x4 r;
  r[0] = __builtin_amdgcn_readfirstlane((int)(unsigned)a);
  r[1] = __builtin_amdgcn_readfirstlane((int)((unsigned)(a >> 32) & 0xffffu));
  r[2] = (int)bytes;
  r[3] = 0x00020000;
  return r;
}
__device__ __forceinline__ void dma16(i32x4 rsrc, unsigned lds_addr, unsigned voff, unsigned soff) {
  asm volatile("s_mov_b32 m0, %0\n\ts_nop 0\n\tbuffer_load_dwordx4 %1, %2, %3 offen lds" ::"s"(lds_addr), "v"(voff), "s"(rsrc), "s"(soff));
}
template <int N> __device__ __forceinline__ void wait_vm() { asm volatile("s_waitcnt vmcnt(%0)" ::"n"(N) : "memory"); }

template <int BM, int BN, int S, int NST, int WM, int WN>
__global__ __launch_bounds__(WM* WN * 64) void gemm_kernel(const int* __restrict__ idx, const unsigned short* __restrict__ table,
                                                           const unsigned short* __restrict__ W1, const unsigned short* __restrict__ b1,
                                                           const unsigned short* __restrict__ W2, float* __restrict__ ws) {
  constexpr int NW = WM * WN, NT = NW * 64;
  constexpr int KF = kF / S;
  constexpr int MT = kB / BM, NTL = kH / BN;
  constexpr int A_BYTES = BM * 128, B_BYTES = BN * 128, ST_BYTES = A_BYTES + B_BYTES;
  constexpr int PA = BM / (8 * NW), PB = BN / (8 * NW), P = PA + PB;
  static_assert(PA >= 1 && PB >= 1, "pieces");
  constexpr int TM = BM / WM, TN = BN / WN, FM = TM / 16, FN = TN / 16;
  constexpr int KFP = KF | 1;
  extern __shared__ __attribute__((aligned(16))) char smem[];
  int* sidx = (int*)(smem + NST * ST_BYTES);

  const int tid = threadIdx.x, lane = tid & 63;
  const int w = __builtin_amdgcn_readfirstlane(tid >> 6);
  const int b = blockIdx.x, xcd = b & 7, j = b >> 3;
  int mt, nt, ks;
  if (S == 1 || !CFG_KXCD) {
    constexpr int per = NTL * S;
    mt = xcd * (MT / 8) + j / per;
    nt = (j % per) % NTL;
    ks = (j % per) / NTL;
  } else {
    constexpr int MG = 8 / S;
    ks = xcd % S;
    mt = (xcd / S) * (MT / MG) + j / NTL;
    nt = j % NTL;
  }
  const int m0 = mt * BM, n0 = nt * BN, f0 = ks * KF;

  const i32x4 rT = mk_rsrc(table, kV * 128);
  const i32x4 rW = mk_rsrc(W1, kF * kD * kH * 2);
  const unsigned lds0 = (unsigned)(size_t)(lds_ptr_t)smem;

  int a_c16[PA];
  int a_row[PA];
#pragma unroll
  for (int i = 0; i < PA; ++i) {
    int p = w * PA + i;
    int r = 8 * p + (lane >> 3);
    a_row[i] = r;
    a_c16[i] = (((lane & 7) ^ ((r >> 1) & 7)) << 4);
  }
  int b_voff[PB];
#pragma unroll
  for (int i = 0; i < PB; ++i) {
    int q = w * PB + i;
    int nb = q >> 3, kr = 8 * (q & 7) + (lane >> 3);
    int F = (((kr >> 1) & 1) << 1) | ((kr >> 3) & 1);
    int c = (lane & 7) ^ (2 * F);
    b_voff[i] = (kr * kH + n0 + 64 * nb) * 2 + c * 16;
  }

  auto issueB = [&](int kf, int slot) {
    if (ABL_NOB) return;
#pragma unroll
    for (int i = 0; i < PB; ++i) {
      dma16(rW, lds0 + slot * ST_BYTES + A_BYTES + (w * PB + i) * 1024, b_voff[i], (f0 + kf) * (64 * kH * 2));
    }
  };
  auto issueA = [&](const int (&ix)[PA], int slot) {
    if (ABL_NOA) return;
#pragma unroll
    for (int i = 0; i < PA; ++i) {
      dma16(rT, lds0 + slot * ST_BYTES + (w * PA + i) * 1024, ix[i] * 128 + a_c16[i], 0);
    }
  };
  constexpr int PE = (ABL_NOA ? 0 : PA) + (ABL_NOB ? 0 : PB);

#pragma unroll
  for (int s = 0; s < NST - 1; ++s) issueB(s, s);
  for (int e = tid; e < BM * KF; e += NT) {
    int r = e / KF, f = e - r * KF;
    sidx[r * KFP + f] = idx[(m0 + r) * kF + f0 + f];
  }
  wait_vm<0>();
  __syncthreads();
#pragma unroll
  for (int s = 0; s < NST - 1; ++s) {
    int ix[PA];
#pragma unroll
    for (int i = 0; i < PA; ++i) ix[i] = sidx[a_row[i] * KFP + s];
    issueA(ix, s);
  }

  const int wm = w / WN, wn = w % WN;
  const int fr = lane & 15, fq = lane >> 4;
  int a_rd[2];
#pragma unroll
  for (int s = 0; s < 2; ++s) {
    int row = wm * TM + fr;
    a_rd[s] = row * 128 + (((4 * s + fq) ^ ((row >> 1) & 7)) << 4);
  }
  int b_rd[2];
  {
    int q = (lane & 15) >> 2, p = lane & 3;
#pragma unroll
    for (int t = 0; t < 2; ++t) {
      int kr = 8 * fq + 4 * t + q;
      int F = (((kr >> 1) & 1) << 1) | ((kr >> 3) & 1);
      b_rd[t] = kr * 128 + ((F * 2) << 4) + (p >> 1) * 16 + (p & 1) * 8;
    }
  }

  f32x4 acc[FN][FM];
#pragma unroll
  for (int a = 0; a < FN; ++a)
#pragma unroll
    for (int c = 0; c < FM; ++c) acc[a][c] = (f32x4){0.f, 0.f, 0.f, 0.f};

  int ixn[PA];
#pragma unroll
  for (int i = 0; i < PA; ++i) ixn[i] = sidx[a_row[i] * KFP + (NST - 1 < KF ? NST - 1 : 0)];

  auto compute = [&](int slot) {
    const char* sA = smem + slot * ST_BYTES;
    const char* sB = sA + A_BYTES;
#pragma unroll
    for (int s = 0; s < 2; ++s) {
      bf16x8 ea[FM];
#pragma unroll
      for (int i = 0; i < FM; ++i) ea[i] = *(const bf16x8*)(sA + a_rd[s] + i * 2048);
      bf16x8 wb[FN];
#pragma unroll
      for (int a = 0; a < FN; ++a) {
        int col = wn * TN + a * 16;
        int nb = col >> 6, cb = (col >> 4) & 3;
        const char* base = sB + nb * 8192 + s * 4096;
        bf16x4 lo = __builtin_amdgcn_ds_read_tr16_b64_v4i16((__attribute__((address_space(3))) bf16x4*)(base + (b_rd[0] ^ (cb << 5))));
        bf16x4 hi = __builtin_amdgcn_ds_read_tr16_b64_v4i16((__attribute__((address_space(3))) bf16x4*)(base + (b_rd[1] ^ (cb << 5))));
        wb[a] = __builtin_shufflevector(lo, hi, 0, 1, 2, 3, 4, 5, 6, 7);
      }
      if (!ABL_NOMFMA) {
#pragma unroll
        for (int a = 0; a < FN; ++a)
#pragma unroll
          for (int i = 0; i < FM; ++i) acc[a][i] = __builtin_amdgcn_mfma_f32_16x16x32_bf16(wb[a], ea[i], acc[a][i], 0, 0, 0);
      } else {
#pragma unroll
        for (int a = 0; a < FN; ++a) asm volatile("" ::"v"(wb[a]));
#pragma unroll
        for (int i = 0; i < FM; ++i) asm volatile("" ::"v"(ea[i]));
      }
    }
  };

  constexpr int PAE = ABL_NOA ? 0 : PA;
  int kk = 0;
#pragma unroll
  for (int k = 0; k < NST - 1; ++k) {
    if (k == 0) wait_vm<(NST - 2) * PAE>();
    else if (k == 1) wait_vm<(NST - 3 > 0 ? NST - 3 : 0) * PAE + 1 * PE>();
    else if (k == 2) wait_vm<(NST - 4 > 0 ? NST - 4 : 0) * PAE + 2 * PE>();
    else if (k == 3) wait_vm<(NST - 5 > 0 ? NST - 5 : 0) * PAE + 3 * PE>();
    else if (k == 4) wait_vm<(NST - 6 > 0 ? NST - 6 : 0) * PAE + 4 * PE>();
    else if (k == 5) wait_vm<(NST - 7 > 0 ? NST - 7 : 0) * PAE + 5 * PE>();
    else if (k == 6) wait_vm<(NST - 8 > 0 ? NST - 8 : 0) * PAE + 6 * PE>();
    else if (k == 7) wait_vm<(NST - 9 > 0 ? NST - 9 : 0) * PAE + 7 * PE>();
    asm volatile("s_waitcnt lgkmcnt(0)" ::: "memory");
    __builtin_amdgcn_s_barrier();
    const int kn = k + NST - 1;
    const int sn = (k + NST - 1) % NST;
    issueB(kn, sn);
    issueA(ixn, sn);
#pragma unroll
    for (int i = 0; i < PA; ++i) ixn[i] = sidx[a_row[i] * KFP + (kn + 1 < KF ? kn + 1 : 0)];
    compute(k);
  }
  kk = NST - 1;
  int cs = NST - 1;
  for (; kk < KF - (NST - 1); ++kk) {
    wait_vm<(NST - 2) * PE>();
    asm volatile("s_waitcnt lgkmcnt(0)" ::: "memory");
    __builtin_amdgcn_s_barrier();
    const int kn = kk + NST - 1;
    const int sn = cs == 0 ? NST - 1 : cs - 1;
    issueB(kn, sn);
    issueA(ixn, sn);
#pragma unroll
    for (int i = 0; i < PA; ++i) ixn[i] = sidx[a_row[i] * KFP + (kn + 1 < KF ? kn + 1 : 0)];
    compute(cs);
    cs = cs == NST - 1 ? 0 : cs + 1;
  }
#pragma unroll
  for (int t = 0; t < NST - 1; ++t) {
    if (t == 0) wait_vm<(NST - 2) * PE>();
    else if (t == 1) wait_vm<(NST - 3 > 0 ? NST - 3 : 0) * PE>();
    else if (t == 2) wait_vm<(NST - 4 > 0 ? NST - 4 : 0) * PE>();
    else if (t == 3) wait_vm<(NST - 5 > 0 ? NST - 5 : 0) * PE>();
    else if (t == 4) wait_vm<(NST - 6 > 0 ? NST - 6 : 0) * PE>();
    else if (t == 5) wait_vm<(NST - 7 > 0 ? NST - 7 : 0) * PE>();
    else if (t == 6) wait_vm<(NST - 8 > 0 ? NST - 8 : 0) * PE>();
    else if (t == 7) wait_vm<(NST - 9 > 0 ? NST - 9 : 0) * PE>();
    asm volatile("s_waitcnt lgkmcnt(0)" ::: "memory");
    __builtin_amdgcn_s_barrier();
    compute(cs);
    cs = cs == NST - 1 ? 0 : cs + 1;
  }

  if (S == 1) {
    float part[FM];
#pragma unroll
    for (int i = 0; i < FM; ++i) part[i] = 0.f;
#pragma unroll
    for (int a = 0; a < FN; ++a) {
      int n = n0 + wn * TN + a * 16 + fq * 4;
#pragma unroll
      for (int r = 0; r < 4; ++r) {
        float bb = bf2f(b1[n + r]), ww = bf2f(W2[n + r]);
#pragma unroll
        for (int i = 0; i < FM; ++i) {
          float h = rbf(rbf(acc[a][i][r]) + bb);
          h = h > 0.f ? h : 0.f;
          part[i] += h * ww;
        }
      }
    }
#pragma unroll
    for (int i = 0; i < FM; ++i) {
      part[i] += __shfl_xor(part[i], 16);
      part[i] += __shfl_xor(part[i], 32);
    }
    if (fq == 0) {
#pragma unroll
      for (int i = 0; i < FM; ++i) ws[(nt * WN + wn) * kB + m0 + wm * TM + i * 16 + fr] = part[i];
    }
  } else {
#pragma unroll
    for (int a = 0; a < FN; ++a)
#pragma unroll
      for (int i = 0; i < FM; ++i) {
        int m = m0 + wm * TM + i * 16 + fr, n = n0 + wn * TN + a * 16 + fq * 4;
        *(f32x4*)(ws + ((size_t)ks * kB + m) * kH + n) = acc[a][i];
      }
  }
}

template <int NP>
__global__ void final1(const float* __restrict__ ws, const unsigned short* __restrict__ xw, const unsigned short* __restrict__ ww,
                       const unsigned short* __restrict__ bw, const unsigned short* __restrict__ b2, __hip_bfloat16* __restrict__ out) {
  int m = blockIdx.x * blockDim.x + threadIdx.x;
  float d = 0.f;
#pragma unroll
  for (int p = 0; p < NP; ++p) d += ws[p * kB + m];
  d = rbf(rbf(d) + bf2f(b2[0]));
  float wd = 0.f;
#pragma unroll
  for (int i = 0; i < kNd; ++i) wd += bf2f(xw[m * kNd + i]) * bf2f(ww[i]);
  wd = rbf(rbf(wd) + bf2f(bw[0]));
  float z = rbf(wd + d);
  out[m] = __float2bfloat16(1.f / (1.f + __expf(-z)));
}

template <int S>
__global__ void finalS(const float* __restrict__ ws, const unsigned short* __restrict__ b1, const unsigned short* __restrict__ W2,
                       const unsigned short* __restrict__ xw, const unsigned short* __restrict__ ww, const unsigned short* __restrict__ bw,
                       const unsigned short* __restrict__ b2, __hip_bfloat16* __restrict__ out) {
  int lane = threadIdx.x & 63;
  int m = blockIdx.x * (blockDim.x >> 6) + (threadIdx.x >> 6);
  f32x4 s = {0.f, 0.f, 0.f, 0.f};
#pragma unroll
  for (int k = 0; k < S; ++k) s += *(const f32x4*)(ws + ((size_t)k * kB + m) * kH + lane * 4);
  float d = 0.f;
#pragma unroll
  for (int r = 0; r < 4; ++r) {
    float h = rbf(rbf(s[r]) + bf2f(b1[lane * 4 + r]));
    h = h > 0.f ? h : 0.f;
    d += h * bf2f(W2[lane * 4 + r]);
  }
  float wd = lane < kNd ? bf2f(xw[m * kNd + lane]) * bf2f(ww[lane]) : 0.f;
#pragma unroll
  for (int o = 32; o >= 1; o >>= 1) {
    d += __shfl_xor(d, o);
    wd += __shfl_xor(wd, o);
  }
  if (lane == 0) {
    d = rbf(rbf(d) + bf2f(b2[0]));
    wd = rbf(rbf(wd) + bf2f(bw[0]));
    float z = rbf(wd + d);
    out[m] = __float2bfloat16(1.f / (1.f + __expf(-z)));
  }
}

extern "C" void kernel_launch(void* const* d_in, const int* in_sizes, int n_in, void* d_out, int out_size, void* d_ws, size_t ws_size,
                              hipStream_t stream) {
  const int* idx = (const int*)d_in[0];
  const unsigned short* table = (const unsigned short*)d_in[1];
  const unsigned short* xw = (const unsigned short*)d_in[2];
  const unsigned short* ww = (const unsigned short*)d_in[3];
  const unsigned short* bw = (const unsigned short*)d_in[4];
  const unsigned short* W1 = (const unsigned short*)d_in[5];
  const unsigned short* b1 = (const unsigned short*)d_in[6];
  const unsigned short* W2 = (const unsigned short*)d_in[7];
  const unsigned short* b2 = (const unsigned short*)d_in[8];
  float* ws = (float*)d_ws;
  constexpr int BM = CFG_BM, BN = CFG_BN, S = CFG_S, NST = CFG_NST, WM = CFG_WM, WN = CFG_WN;
  constexpr int KF = kF / S;
  constexpr int lds = NST * (BM + BN) * 128 + BM * (KF | 1) * 4;
  static_assert(lds <= 160 * 1024, "lds");
  auto kern = gemm_kernel<BM, BN, S, NST, WM, WN>;
  (void)hipFuncSetAttribute((const void*)kern, hipFuncAttributeMaxDynamicSharedMemorySize, lds);
  constexpr int grid = (kB / BM) * (kH / BN) * S;
  kern<<<grid, WM * WN * 64, lds, stream>>>(idx, table, W1, b1, W2, ws);
  if (S == 1)
    final1<(kH / BN) * WN><<<kB / 256, 256, 0, stream>>>(ws, xw, ww, bw, b2, (__hip_bfloat16*)d_out);
  else
    finalS<S><<<kB / 4, 256, 0, stream>>>(ws, b1, W2, xw, ww, bw, b2, (__hip_bfloat16*)d_out);
}
```

```cpp
#include <hip/hip_runtime.h>
#include <hip/hip_bf16.h>
#include <stdint.h>

typedef __attribute__((ext_vector_type(8))) short bf16x8;
typedef __attribute__((ext_vector_type(4))) short bf16x4;
typedef __attribute__((ext_vector_type(4))) float f32x4;
typedef __attribute__((ext_vector_type(4))) int i32x4;
typedef __attribute__((ext_vector_type(4))) unsigned u32x4;
typedef __attribute__((ext_vector_type(2))) unsigned u32x2;
typedef __attribute__((address_space(3))) void* lds_ptr_t;
typedef __attribute__((address_space(3))) bf16x4* lds_b4_t;
typedef __attribute__((address_space(3))) bf16x8* lds_b8_t;
typedef __attribute__((address_space(3))) i32x4* lds_i4_t;

constexpr int kB = 4096, kF = 100, kD = 64, kV = 100000, kNd = 13, kH = 256;
#define CFG_NA 5
#define CFG_NB 4
#define ROT 3
#define ABL_NOA 0
#define ABL_NOB 0
#define ABL_NORD 0
#define ABL_NOMF 0
#define ABL_NOC 0
constexpr int NA = CFG_NA, NB = CFG_NB, KF = 25;
constexpr int SLOT = 16384;
constexpr int IDX_STRIDE = 528;
constexpr int LDS_B = NA * SLOT;
constexpr int LDS_IDX = (NA + NB) * SLOT;
constexpr int LDS_TOTAL = LDS_IDX + 28 * IDX_STRIDE;
static_assert(LDS_TOTAL <= 160 * 1024, "lds");

__device__ __forceinline__ float bf2f(unsigned short u) { return __uint_as_float(((unsigned)u) << 16); }
__device__ __forceinline__ float bflo(unsigned u) { return __uint_as_float(u << 16); }
__device__ __forceinline__ float bfhi(unsigned u) { return __uint_as_float(u & 0xffff0000u); }
__device__ __forceinline__ float rbf(float x) { return __bfloat162float(__float2bfloat16(x)); }
typedef __attribute__((ext_vector_type(2))) __bf16 bf16v2;
__device__ __forceinline__ unsigned pk2(float a, float b) {
  bf16v2 v; v[0] = (__bf16)a; v[1] = (__bf16)b;
  return __builtin_bit_cast(unsigned, v);
}

__device__ __forceinline__ i32x4 mk_rsrc(const void* p, unsigned bytes) {
  unsigned long long a = (unsigned long long)p;
  i32x4 r;
  r[0] = __builtin_amdgcn_readfirstlane((int)(unsigned)a);
  r[1] = __builtin_amdgcn_readfirstlane((int)((unsigned)(a >> 32) & 0xffffu));
  r[2] = (int)bytes;
  r[3] = 0x00020000;
  return r;
}
__device__ __forceinline__ void dma16(i32x4 rsrc, unsigned lds_addr, unsigned voff, unsigned soff) {
  asm volatile("s_mov_b32 m0, %0\n\ts_nop 0\n\tbuffer_load_dwordx4 %1, %2, %3 offen lds" ::"s"(lds_addr), "v"(voff), "s"(rsrc), "s"(soff));
}
template <int N> __device__ __forceinline__ void wait_vm_bar() { asm volatile("s_waitcnt vmcnt(%0)\n\ts_barrier" ::"n"(N) : "memory"); }
__device__ __forceinline__ void lgkm_bar() {
  __builtin_amdgcn_s_waitcnt(0xC07F);
  asm volatile("s_barrier" ::: "memory");
}
#define STAMP(i)

template <int N, int P, int K> struct Tail {
  static __device__ __forceinline__ void run() {
    wait_vm_bar<(K - 1) * P>();
    Tail<N, P, K - 1>::run();
  }
};
template <int N, int P> struct Tail<N, P, 0> { static __device__ __forceinline__ void run() {} };

__global__ __launch_bounds__(512, 2) void gemm_kernel(const int* __restrict__ idx, const unsigned short* __restrict__ table,
                                                      const unsigned short* __restrict__ W1, unsigned* __restrict__ ws) {
  extern __shared__ __attribute__((aligned(16))) char smem[];
  const unsigned lds0 = (unsigned)(size_t)(lds_ptr_t)smem;
  const int tid = threadIdx.x, lane = tid & 63;
  const int w = __builtin_amdgcn_readfirstlane(tid >> 6);
  const int b = blockIdx.x, xcd = b & 7, j = b >> 3;
  const int ks = xcd & 3;
  const int mt = (xcd >> 2) * 16 + (j >> 1);
  const int nt = j & 1;
  const int m0 = mt * 128, n0 = nt * 128, f0 = ks * KF;
  const int rot = (mt * ROT) % KF;
  STAMP(0);
  if (w >= 6) {
    const int wb = w - 6;
    const i32x4 rW = mk_rsrc(W1, kF * kD * kH * 2);
    int voff[2];
#pragma unroll
    for (int o = 0; o < 2; ++o) {
      int F = (((lane >> 4) & 1) << 1) | o;
      int c = (lane & 7) ^ (2 * F);
      voff[o] = (lane >> 3) * 512 + (n0 + 64 * wb) * 2 + c * 16;
    }
    int kf = rot;
    unsigned dst = lds0 + LDS_B + wb * 8192;
    auto issue = [&]() {
      const unsigned so = (f0 + kf) * 32768;
      if (!ABL_NOB) {
#pragma unroll
        for (int i = 0; i < 8; ++i) dma16(rW, dst + i * 1024, voff[i & 1], so + i * 4096);
      }
      kf = kf == KF - 1 ? 0 : kf + 1;
      dst = dst + SLOT >= lds0 + LDS_B + NB * SLOT ? dst - (NB - 1) * SLOT : dst + SLOT;
    };
#pragma unroll 1
    for (int s = 0; s < NB; ++s) issue();
    asm volatile("s_barrier" ::: "memory");
    wait_vm_bar<(NB - 1) * 8>();
#pragma unroll 1
    for (int k = 1; k <= KF - NB; ++k) {
      wait_vm_bar<(NB - 2) * 8>();
      issue();
    }
    Tail<NB, 8, NB - 1>::run();
  } else if (w >= 4) {
    const int wa = w - 4;
    const i32x4 rT = mk_rsrc(table, kV * 128);
    int c16[2];
#pragma unroll
    for (int o = 0; o < 2; ++o) c16[o] = ((lane & 7) ^ ((4 * o + (lane >> 4)) & 7)) << 4;
    int kf = rot;
    unsigned dst = lds0 + wa * 8192;
    const unsigned ia = lds0 + LDS_IDX + wa * 256 + (lane >> 3) * 16;
    i32x4 ix0, ix1;
    auto fetch = [&]() {
      ix0 = *(lds_i4_t)(size_t)(ia + kf * IDX_STRIDE);
      ix1 = *(lds_i4_t)(size_t)(ia + kf * IDX_STRIDE + 128);
      kf = kf == KF - 1 ? 0 : kf + 1;
    };
    auto issue = [&]() {
      if (!ABL_NOA) {
#pragma unroll
        for (int i = 0; i < 4; ++i) dma16(rT, dst + i * 1024, ix0[i] + c16[i & 1], 0);
#pragma unroll
        for (int i = 0; i < 4; ++i) dma16(rT, dst + 4096 + i * 1024, ix1[i] + c16[i & 1], 0);
      }
      dst = dst + SLOT >= lds0 + NA * SLOT ? dst - (NA - 1) * SLOT : dst + SLOT;
    };
    asm volatile("s_barrier" ::: "memory");
    fetch();
#pragma unroll 1
    for (int s = 0; s < NA; ++s) { issue(); fetch(); }
    wait_vm_bar<(NA - 1) * 8>();
#pragma unroll 1
    for (int k = 1; k <= KF - NA; ++k) {
      wait_vm_bar<(NA - 2) * 8>();
      issue();
      fetch();
    }
    Tail<NA, 8, NA - 1>::run();
  } else {
    {
      const __amdgpu_buffer_rsrc_t rI = __builtin_amdgcn_make_buffer_rsrc((void*)idx, 0, kB * kF * 4, 0x00020000);
      u32x4 iv[4];
      const int q = lane >> 3, jj = lane & 7;
#pragma unroll
      for (int i = 0; i < 4; ++i) iv[i] = __builtin_amdgcn_raw_buffer_load_b128(rI, ((m0 + w * 32 + 8 * i + q) * kF + f0 + jj * 4) * 4, 0, 0);
      if (jj < 7) {
#pragma unroll
        for (int c = 0; c < 4; ++c) {
          i32x4 v = {(int)(iv[0][c] << 7), (int)(iv[1][c] << 7), (int)(iv[2][c] << 7), (int)(iv[3][c] << 7)};
          *(lds_i4_t)(size_t)(lds0 + LDS_IDX + (jj * 4 + c) * IDX_STRIDE + w * 128 + q * 16) = v;
        }
      }
    }
    lgkm_bar();
    STAMP(1);
    const int wm = w >> 1, wn = w & 1;
    const int fr = lane & 15, fq = lane >> 4;
    unsigned a_rd[2], b_rd[2];
#pragma unroll
    for (int s = 0; s < 2; ++s) {
      int row = wm * 64 + fr;
      a_rd[s] = lds0 + row * 128 + (((4 * s + fq) ^ ((row >> 1) & 7)) << 4);
    }
    {
      int q = (lane & 15) >> 2, p = lane & 3;
#pragma unroll
      for (int t = 0; t < 2; ++t) {
        int kr = 8 * fq + 4 * t + q;
        int F = (((kr >> 1) & 1) << 1) | ((kr >> 3) & 1);
        b_rd[t] = lds0 + LDS_B + wn * 8192 + kr * 128 + ((F * 2) << 4) + (p >> 1) * 16 + (p & 1) * 8;
      }
    }
    f32x4 acc[4][4];
    bf16x8 ea[2][4], wf[2][4];
#pragma unroll
    for (int a = 0; a < 4; ++a) {
#pragma unroll
      for (int c = 0; c < 4; ++c) acc[a][c] = (f32x4){0.f, 0.f, 0.f, 0.f};
      ea[1][a] = (bf16x8){0, 0, 0, 0, 0, 0, 0, 0};
      wf[1][a] = (bf16x8){0, 0, 0, 0, 0, 0, 0, 0};
      if (ABL_NORD) { ea[0][a] = (bf16x8){1, 2, 3, 4, (short)lane, (short)(lane*77), 7, 8}; wf[0][a] = ea[0][a]; ea[1][a] = ea[0][a]; wf[1][a] = ea[0][a]; }
    }
    unsigned sa = 0, sb = 0;
    auto reads = [&](int s) {
      if (ABL_NORD) {
#pragma unroll
        for (int i = 0; i < 4; ++i) { asm volatile("" : "+v"(ea[s][i])); asm volatile("" : "+v"(wf[s][i])); }
        return;
      }
#pragma unroll
      for (int i = 0; i < 4; ++i) {
        ea[s][i] = *(lds_b8_t)(size_t)(a_rd[s] + sa + i * 2048);
        bf16x4 lo = __builtin_amdgcn_ds_read_tr16_b64_v4i16((lds_b4_t)(size_t)(((b_rd[0] + sb) ^ (i << 5)) + s * 4096));
        bf16x4 hi = __builtin_amdgcn_ds_read_tr16_b64_v4i16((lds_b4_t)(size_t)(((b_rd[1] + sb) ^ (i << 5)) + s * 4096));
        wf[s][i] = __builtin_shufflevector(lo, hi, 0, 1, 2, 3, 4, 5, 6, 7);
      }
    };
    auto mfmas = [&](int s) {
      if (ABL_NOMF) {
#pragma unroll
        for (int i = 0; i < 4; ++i) { asm volatile("" :: "v"(ea[s][i])); asm volatile("" :: "v"(wf[s][i])); }
        return;
      }
#pragma unroll
      for (int a = 0; a < 4; ++a)
#pragma unroll
        for (int i = 0; i < 4; ++i) acc[a][i] = __builtin_amdgcn_mfma_f32_16x16x32_bf16(wf[s][a], ea[s][i], acc[a][i], 0, 0, 0);
    };
    auto sched = [&]() {
#pragma unroll
      for (int g = 0; g < 12; ++g) {
        __builtin_amdgcn_sched_group_barrier(0x100, 1, 0);
        __builtin_amdgcn_sched_group_barrier(0x8, 1, 0);
      }
      __builtin_amdgcn_sched_group_barrier(0x8, 4, 0);
    };
#pragma unroll 1
    for (int k = 0; k < KF; ++k) {
      lgkm_bar();
      if (!ABL_NOC) {
        reads(0);
        mfmas(1);
        sched();
        __builtin_amdgcn_sched_barrier(0);
        reads(1);
        mfmas(0);
        sched();
        __builtin_amdgcn_sched_barrier(0);
      }
      sa = sa == (NA - 1) * SLOT ? 0 : sa + SLOT;
      sb = sb == (NB - 1) * SLOT ? 0 : sb + SLOT;
    }
    mfmas(1);
    STAMP(3);
    const __amdgpu_buffer_rsrc_t rP = __builtin_amdgcn_make_buffer_rsrc((void*)ws, 0, 32 * 8 * 32768, 0x00020000);
#pragma unroll
    for (int i = 0; i < 4; ++i)
#pragma unroll
      for (int h = 0; h < 2; ++h) {
        u32x4 v = {pk2(acc[2 * h][i][0], acc[2 * h][i][1]), pk2(acc[2 * h][i][2], acc[2 * h][i][3]),
                   pk2(acc[2 * h + 1][i][0], acc[2 * h + 1][i][1]), pk2(acc[2 * h + 1][i][2], acc[2 * h + 1][i][3])};
        const int g = wm * 4 + i;
        __builtin_amdgcn_raw_buffer_store_b128(v, rP, lane * 16, (((mt * 8 + g) * 4 + ks) * 8 + nt * 4 + wn * 2 + h) * 1024, 16);
      }
  }
}

__global__ __launch_bounds__(512) void final_kernel(const unsigned* __restrict__ ws, const unsigned short* __restrict__ b1, const unsigned short* __restrict__ W2,
                       const unsigned short* __restrict__ xw, const unsigned short* __restrict__ ww, const unsigned short* __restrict__ bw,
                       const unsigned short* __restrict__ b2, __hip_bfloat16* __restrict__ out) {
  __shared__ float red[8][16];
  const int tid = threadIdx.x, lane = tid & 63, c = tid >> 6;
  const int fr = lane & 15, fq = lane >> 4;
  const int G = blockIdx.x;
  u32x4 p[4];
#pragma unroll
  for (int k = 0; k < 4; ++k) p[k] = *(const u32x4*)(ws + ((G * 4 + k) * 8 + c) * 256 + lane * 4);
  u32x2 bb[2], w2[2];
#pragma unroll
  for (int h = 0; h < 2; ++h) {
    bb[h] = *(const u32x2*)(b1 + c * 32 + h * 16 + fq * 4);
    w2[h] = *(const u32x2*)(W2 + c * 32 + h * 16 + fq * 4);
  }
  float wd = 0.f;
  if (tid < 16) {
#pragma unroll
    for (int i = 0; i < kNd; ++i) wd += bf2f(xw[(G * 16 + tid) * kNd + i]) * bf2f(ww[i]);
    wd = rbf(rbf(wd) + bf2f(bw[0]));
  }
  float d = 0.f;
#pragma unroll
  for (int e = 0; e < 4; ++e) {
    float lo = (bflo(p[0][e]) + bflo(p[1][e])) + (bflo(p[2][e]) + bflo(p[3][e]));
    float hi = (bfhi(p[0][e]) + bfhi(p[1][e])) + (bfhi(p[2][e]) + bfhi(p[3][e]));
    unsigned bv = bb[e >> 1][e & 1], wv = w2[e >> 1][e & 1];
    lo = fmaxf(rbf(rbf(lo) + bflo(bv)), 0.f);
    hi = fmaxf(rbf(rbf(hi) + bfhi(bv)), 0.f);
    d += lo * bflo(wv);
    d += hi * bfhi(wv);
  }
  d += __shfl_xor(d, 16);
  d += __shfl_xor(d, 32);
  if (lane < 16) red[c][lane] = d;
  __syncthreads();
  if (tid < 16) {
    float s = 0.f;
#pragma unroll
    for (int k = 0; k < 8; ++k) s += red[k][tid];
    s = rbf(rbf(s) + bf2f(b2[0]));
    float z = rbf(wd + s);
    out[G * 16 + tid] = __float2bfloat16(1.f / (1.f + __expf(-z)));
  }
}

extern "C" void kernel_launch(void* const* d_in, const int* in_sizes, int n_in, void* d_out, int out_size, void* d_ws, size_t ws_size,
                              hipStream_t stream) {
  const int* idx = (const int*)d_in[0];
  const unsigned short* table = (const unsigned short*)d_in[1];
  const unsigned short* xw = (const unsigned short*)d_in[2];
  const unsigned short* ww = (const unsigned short*)d_in[3];
  const unsigned short* bw = (const unsigned short*)d_in[4];
  const unsigned short* W1 = (const unsigned short*)d_in[5];
  const unsigned short* b1 = (const unsigned short*)d_in[6];
  const unsigned short* W2 = (const unsigned short*)d_in[7];
  const unsigned short* b2 = (const unsigned short*)d_in[8];
  unsigned* ws = (unsigned*)d_ws;
  (void)hipFuncSetAttribute((const void*)gemm_kernel, hipFuncAttributeMaxDynamicSharedMemorySize, LDS_TOTAL);
  gemm_kernel<<<256, 512, LDS_TOTAL, stream>>>(idx, table, W1, ws);
  final_kernel<<<256, 512, 0, stream>>>(ws, b1, W2, xw, ww, bw, b2, (__hip_bfloat16*)d_out);
}
```

```cpp
#include <hip/hip_runtime.h>
#include <hip/hip_bf16.h>
#include <stdint.h>

typedef __attribute__((ext_vector_type(8))) short bf16x8;
typedef __attribute__((ext_vector_type(4))) short bf16x4;
typedef __attribute__((ext_vector_type(4))) float f32x4;
typedef __attribute__((ext_vector_type(4))) int i32x4;
typedef __attribute__((ext_vector_type(4))) unsigned u32x4;
typedef __attribute__((ext_vector_type(2))) unsigned u32x2;
#define LAS __attribute__((address_space(3)))
typedef LAS char* lds_cp;

constexpr int kB = 4096, kF = 100, kD = 64, kV = 100000, kNd = 13, kH = 256;
#define CFG_NA 5
#define CFG_NB 4
#define ROT 3
#define SCHED 1
#define SWAPB 4
#define LIN 0
#define TLBW 0
#define WRAMP 4
#define PREW 0
#define USE_B0 1
#define RAMP 1
#define ABL_NOA 0
#define ABL_NOB 0
#define ABL_NOC 0
constexpr int NA = CFG_NA, NB = CFG_NB, KF = 25;
constexpr int SLOT = 16384;
constexpr int IDX_STRIDE = 528;
constexpr int LDS_B = NA * SLOT;
constexpr int LDS_IDX = (NA + NB) * SLOT;
constexpr int LDS_PAR = LDS_IDX + (KF + 1) * IDX_STRIDE;
constexpr int LDS_TOTAL = LDS_PAR + 1024 + 64 + 16;
static_assert(LDS_TOTAL <= 160 * 1024, "lds");
constexpr int INBOX_BYTES = 256 * 32768;
constexpr int WS_BYTES = INBOX_BYTES + 256 * 256;
constexpr unsigned kSpinCap = 1u << 16;

__device__ __forceinline__ float bf2f(unsigned short u) { return __uint_as_float(((unsigned)u) << 16); }
__device__ __forceinline__ float bflo(unsigned u) { return __uint_as_float(u << 16); }
__device__ __forceinline__ float bfhi(unsigned u) { return __uint_as_float(u & 0xffff0000u); }
__device__ __forceinline__ float rbf(float x) { return (float)(__bf16)x; }
typedef __attribute__((ext_vector_type(2))) __bf16 bf16v2;
typedef __attribute__((ext_vector_type(2))) float f32x2;
typedef __attribute__((ext_vector_type(2))) short i16x2;
__device__ __forceinline__ f32x2 unp(unsigned u) { return (f32x2){bflo(u), bfhi(u)}; }
__device__ __forceinline__ unsigned pkv(f32x2 t) {
  unsigned r;
  asm("v_cvt_pk_bf16_f32 %0, %1, %2" : "=v"(r) : "v"(t[0]), "v"(t[1]));
  return r;
}
__device__ __forceinline__ unsigned pk2(float a, float b) {
  bf16v2 v; v[0] = (__bf16)a; v[1] = (__bf16)b;
  return __builtin_bit_cast(unsigned, v);
}
__device__ __forceinline__ float sum16(float x) {
  x += __builtin_bit_cast(float, __builtin_amdgcn_update_dpp(0, __builtin_bit_cast(int, x), 0x124, 0xf, 0xf, false));
  x += __builtin_bit_cast(float, __builtin_amdgcn_update_dpp(0, __builtin_bit_cast(int, x), 0x128, 0xf, 0xf, false));
  unsigned u = __float_as_uint(x);
  auto s16 = __builtin_amdgcn_permlane16_swap(u, u, false, false);
  x = __uint_as_float(s16[0]) + __uint_as_float(s16[1]);
  u = __float_as_uint(x);
  auto s32 = __builtin_amdgcn_permlane32_swap(u, u, false, false);
  return __uint_as_float(s32[0]) + __uint_as_float(s32[1]);
}

__device__ __forceinline__ i32x4 mk_rsrc(const void* p, unsigned bytes) {
  unsigned long long a = (unsigned long long)p;
  i32x4 r;
  r[0] = __builtin_amdgcn_readfirstlane((int)(unsigned)a);
  r[1] = __builtin_amdgcn_readfirstlane((int)((unsigned)(a >> 32) & 0xffffu));
  r[2] = (int)bytes;
  r[3] = 0x00020000;
  return r;
}
__device__ __forceinline__ void dma16(i32x4 rsrc, unsigned lds_addr, unsigned voff, unsigned soff) {
  asm volatile("s_mov_b32 m0, %0\n\ts_nop 0\n\tbuffer_load_dwordx4 %1, %2, %3 offen lds" ::"s"(lds_addr), "v"(voff), "s"(rsrc), "s"(soff));
}
template <int N> __device__ __forceinline__ void wait_vm_bar() { asm volatile("s_waitcnt vmcnt(%0)\n\ts_barrier" ::"n"(N) : "memory"); }
__device__ __forceinline__ void bar() { asm volatile("s_barrier" ::: "memory"); }
__device__ __forceinline__ void lgkm_bar() {
  __builtin_amdgcn_s_waitcnt(0xC07F);
  asm volatile("s_barrier" ::: "memory");
}
#define STAMP(i)
#define STAMP_OUT(lo, hi)

template <int P, int K> struct Tail {
  static __device__ __forceinline__ void run() {
    wait_vm_bar<(K - 1) * P>();
    Tail<P, K - 1>::run();
  }
};
template <int P> struct Tail<P, 0> { static __device__ __forceinline__ void run() {} };

__global__ __launch_bounds__(512, 2) void fused_kernel(const int* __restrict__ idx, const unsigned short* __restrict__ table,
                                                       const unsigned short* __restrict__ W1, unsigned* __restrict__ ws,
                                                       const unsigned short* __restrict__ b1, const unsigned short* __restrict__ W2,
                                                       const unsigned short* __restrict__ xw, const unsigned short* __restrict__ ww,
                                                       const unsigned short* __restrict__ bw, const unsigned short* __restrict__ b2,
                                                       __hip_bfloat16* __restrict__ out) {
  extern __shared__ __attribute__((aligned(16))) char smem[];
  const lds_cp sm = (lds_cp)smem;
  const unsigned lds0 = (unsigned)(size_t)sm;
  const int tid = threadIdx.x, lane = tid & 63;
  const int w = __builtin_amdgcn_readfirstlane(tid >> 6);
  const int b = blockIdx.x, xcd = b & 7, j = b >> 3;
  const int ks = xcd & 3;
  const int mt = (xcd >> 2) * 16 + (j >> 1);
  const int nt = j & 1;
  const int m0 = mt * 128, n0 = nt * 128, f0 = ks * KF;
  const int G = mt * 8 + ks * 2 + nt;
  const int rot = (mt * ROT) % KF;
  auto fld = [&](int k) {
    int i = k;
    if (nt && k < (KF / (2 * SWAPB)) * 2 * SWAPB) i = ((k / SWAPB) ^ 1) * SWAPB + k % SWAPB;
    i += rot;
    return i >= KF ? i - KF : i;
  };
  STAMP(0);
  const __amdgpu_buffer_rsrc_t rP = __builtin_amdgcn_make_buffer_rsrc((void*)ws, 0, WS_BYTES, 0x00020000);
  u32x4 iv[8];
  if ((w & 6) == 4) {
    const __amdgpu_buffer_rsrc_t rI = __builtin_amdgcn_make_buffer_rsrc((void*)idx, 0, kB * kF * 4, 0x00020000);
#pragma unroll
    for (int i = 0; i < 8; ++i)
      iv[i] = __builtin_amdgcn_raw_buffer_load_b128(rI, ((m0 + (w - 4) * 64 + 8 * i + (lane >> 3)) * kF + f0 + (lane & 7) * 4) * 4, 0, 0);
  }
  bar();
  if (w < 4) {
    const int wm = w >> 1, wn = w & 1;
    unsigned t0 = 0, t1 = 0, t2 = 0;
    {
      const __amdgpu_buffer_rsrc_t rT = __builtin_amdgcn_make_buffer_rsrc((void*)table, 0, kV * 128, 0x00020000);
      const __amdgpu_buffer_rsrc_t rO = __builtin_amdgcn_make_buffer_rsrc((void*)out, 0, kB * 2, 0x00020000);
      if (TLBW & 1) t0 = __builtin_amdgcn_raw_buffer_load_b32(rT, lane * 262144 + j * 128 + w * 0x4000000, 0, 0);
      if (TLBW & 2) t1 = __builtin_amdgcn_raw_buffer_load_b32(rP, (mt * 8 + (lane & 7)) * 32768 + w * 0x4000000, 0, 16);
      if (TLBW & 4) t2 = __builtin_amdgcn_raw_buffer_load_b32(rO, G * 32 + w * 0x4000000, 0, 16);
    }
    const u32x2 eo = __builtin_amdgcn_raw_buffer_load_b64(rP, INBOX_BYTES + (mt * 8 + wm * 4 + (lane & 3)) * 256, 0, 16);
    {
      const int rl = lane & 3, seg = lane >> 2;
      const unsigned short xv = xw[(G * 16 + w * 4 + rl) * kNd + (seg < kNd ? seg : 0)];
      const unsigned short wv = ww[seg < kNd ? seg : 0];
      const unsigned short bwv = bw[0], b2v = b2[0];
      const u32x2 par = *(const u32x2*)((lane < 32 ? b1 : W2) + (w & 1) * 128 + (lane & 31) * 4);
      const u32x2 em = __builtin_amdgcn_raw_buffer_load_b64(rP, INBOX_BYTES + G * 256, 0, 16);
      float wd = seg < kNd ? bf2f(xv) * bf2f(wv) : 0.f;
      wd = sum16(wd);
      wd = rbf(rbf(wd) + bf2f(bwv));
      *(LAS u32x2*)(sm + LDS_PAR + (lane >> 5) * 512 + (w & 1) * 256 + (lane & 31) * 8) = par;
      *(LAS float*)(sm + LDS_PAR + 1024 + (w * 4 + rl) * 4) = wd;
      *(LAS float*)(sm + LDS_PAR + 1088) = bf2f(b2v);
      *(LAS u32x2*)(sm + LDS_PAR + 1096) = em;
    }
    asm volatile("s_waitcnt vmcnt(0)" ::"v"(t0), "v"(t1), "v"(t2));
    const int fr = lane & 15, fq = lane >> 4;
    lds_cp pa[2], pb[4];
#pragma unroll
    for (int s = 0; s < 2; ++s) {
      int row = wm * 64 + fr;
      pa[s] = sm + row * 128 + (((4 * s + fq) ^ ((row >> 1) & 7)) << 4);
    }
    {
      int q = (lane & 15) >> 2, p = lane & 3;
      int kr = 8 * fq + q;
      int F = (((kr >> 1) & 1) << 1) | ((kr >> 3) & 1);
#pragma unroll
      for (int i = 0; i < 4; ++i) pb[i] = sm + LDS_B + wn * 8192 + kr * 128 + (((F ^ i) * 2) << 4) + (p >> 1) * 16 + (p & 1) * 8;
    }
    f32x4 acc[4][4];
    bf16x8 ea[2][4], wf[2][4];
#pragma unroll
    for (int a = 0; a < 4; ++a) {
#pragma unroll
      for (int c = 0; c < 4; ++c) acc[a][c] = (f32x4){0.f, 0.f, 0.f, 0.f};
      ea[1][a] = (bf16x8){0, 0, 0, 0, 0, 0, 0, 0};
      wf[1][a] = (bf16x8){0, 0, 0, 0, 0, 0, 0, 0};
    }
    int sa = 0, sb = 0;
    auto rd_w = [&](int s, int i) {
      bf16x4 lo = __builtin_amdgcn_ds_read_tr16_b64_v4i16((LAS bf16x4*)(pb[i] + s * 4096));
      bf16x4 hi = __builtin_amdgcn_ds_read_tr16_b64_v4i16((LAS bf16x4*)(pb[i] + s * 4096 + 512));
      wf[s][i] = __builtin_shufflevector(lo, hi, 0, 1, 2, 3, 4, 5, 6, 7);
    };
    auto reads = [&](int s) {
#pragma unroll
      for (int i = 0; i < 4; ++i) {
        ea[s][i] = *(LAS bf16x8*)(pa[s] + i * 2048);
        rd_w(s, i);
      }
    };
    auto mfmas = [&](int s) {
#pragma unroll
      for (int a = 0; a < 4; ++a)
#pragma unroll
        for (int i = 0; i < 4; ++i) acc[a][i] = __builtin_amdgcn_mfma_f32_16x16x32_bf16(wf[s][a], ea[s][i], acc[a][i], 0, 0, 0);
    };
    auto sched = [&](int half) {
#pragma unroll
      for (int g = 0; g < 4; ++g) {
        __builtin_amdgcn_sched_group_barrier(0x8, 1, 0);
        __builtin_amdgcn_sched_group_barrier(0x100, 1, 0);
        __builtin_amdgcn_sched_group_barrier(0x8, 1, 0);
        __builtin_amdgcn_sched_group_barrier(0x100, 2, 0);
      }
#pragma unroll
      for (int g = 0; g < 8; ++g) {
        __builtin_amdgcn_sched_group_barrier(0x8, 1, 0);
        __builtin_amdgcn_sched_group_barrier(0x2, 1, 0);
      }
    };
#pragma unroll 1
    for (int k = 0; k < KF; ++k) {
      lgkm_bar();
      if (!ABL_NOC) {
        reads(0);
        mfmas(1);
        sched(0);
        __builtin_amdgcn_sched_barrier(0);
        reads(1);
        mfmas(0);
        const int da = sa == NA - 1 ? -(NA - 1) * SLOT : SLOT;
        const int db = sb == NB - 1 ? -(NB - 1) * SLOT : SLOT;
        sa = sa == NA - 1 ? 0 : sa + 1;
        sb = sb == NB - 1 ? 0 : sb + 1;
        pa[0] += da; pa[1] += da;
#pragma unroll
        for (int i = 0; i < 4; ++i) pb[i] += db;
        sched(1);
        __builtin_amdgcn_sched_barrier(0);
      }
    }
    mfmas(1);
    STAMP(3);
    const int so = LIN ? lane * 16 : (fr >> 2) * 256 + fq * 64 + (fr & 3) * 16;
#pragma unroll
    for (int i = 0; i < 4; ++i)
#pragma unroll
      for (int h = 0; h < 2; ++h) {
        u32x4 v = {pk2(acc[2 * h][i][0], acc[2 * h][i][1]), pk2(acc[2 * h][i][2], acc[2 * h][i][3]),
                   pk2(acc[2 * h + 1][i][0], acc[2 * h + 1][i][1]), pk2(acc[2 * h + 1][i][2], acc[2 * h + 1][i][3])};
        const int g = wm * 4 + i;
        __builtin_amdgcn_raw_buffer_store_b128(v, rP, so, (((mt * 8 + g) * 4 + ks) * 8 + nt * 4 + wn * 2 + h) * 1024, 16);
      }
    const unsigned long long en = (((unsigned long long)eo[1] << 32) | eo[0]) + 1ull;
    const u32x2 fl = {(unsigned)en, (unsigned)(en >> 32)};
    asm volatile("s_waitcnt vmcnt(0)" ::: "memory");
    if (lane < 4) __builtin_amdgcn_raw_buffer_store_b64(fl, rP, INBOX_BYTES + (mt * 8 + wm * 4 + lane) * 256 + 128 + ((nt * 4 + ks) * 2 + wn) * 8, 0, 16);
    STAMP(4);
    if (w == 0) { STAMP_OUT(0, 1); STAMP_OUT(2, 5); }
  } else {
    const int p = w - 4;
    const int rl = lane & 3, seg = lane >> 2;
    if (w >= 6) {
      const int wb = w - 6;
      const i32x4 rW = mk_rsrc(W1, kF * kD * kH * 2);
      int voff[2];
#pragma unroll
      for (int o = 0; o < 2; ++o) {
        int F = (((lane >> 4) & 1) << 1) | o;
        int c = (lane & 7) ^ (2 * F);
        voff[o] = (lane >> 3) * 512 + (n0 + 64 * wb) * 2 + c * 16;
      }
      int kk = 0;
      unsigned dst = lds0 + LDS_B + wb * 8192;
      auto issue = [&]() {
        const unsigned so = (f0 + fld(kk)) * 32768;
        if (!ABL_NOB) {
#pragma unroll
          for (int i = 0; i < 8; ++i) dma16(rW, dst + i * 1024, voff[i & 1], so + i * 4096);
        }
        ++kk;
        dst = dst + SLOT >= lds0 + LDS_B + NB * SLOT ? dst - (NB - 1) * SLOT : dst + SLOT;
      };
#pragma unroll 1
      for (int s = 0; s < NB; ++s) {
        if (s == WRAMP) wait_vm_bar<(WRAMP - 1) * 8>();
        issue();
      }
      if (WRAMP == NB) wait_vm_bar<(NB - 1) * 8>();
#pragma unroll 1
      for (int k = 1; k <= KF - NB; ++k) {
        wait_vm_bar<(NB - 2) * 8>();
        issue();
      }
      Tail<8, NB - 1>::run();
    } else {
      const int wa = w - 4;
      const i32x4 rT = mk_rsrc(table, kV * 128);
      int c16[2];
#pragma unroll
      for (int o = 0; o < 2; ++o) c16[o] = ((lane & 7) ^ ((4 * o + (lane >> 4)) & 7)) << 4;
      int kk = 0;
      unsigned dst = lds0 + wa * 8192;
      const lds_cp ia = sm + LDS_IDX + wa * 256 + (lane >> 3) * 16;
      i32x4 ix0, ix1;
      auto fetch = [&]() {
        const int kf = fld(kk < KF ? kk : 0);
        ix0 = *(LAS i32x4*)(ia + kf * IDX_STRIDE);
        ix1 = *(LAS i32x4*)(ia + kf * IDX_STRIDE + 128);
        ++kk;
      };
      auto issue = [&]() {
        if (!ABL_NOA) {
#pragma unroll
          for (int i = 0; i < 4; ++i) dma16(rT, dst + i * 1024, ix0[i] + c16[i & 1], 0);
#pragma unroll
          for (int i = 0; i < 4; ++i) dma16(rT, dst + 4096 + i * 1024, ix1[i] + c16[i & 1], 0);
        }
        dst = dst + SLOT >= lds0 + NA * SLOT ? dst - (NA - 1) * SLOT : dst + SLOT;
      };
      {
        const int q = lane >> 3, jj = lane & 7;
#pragma unroll
        for (int hf = 0; hf < 2; ++hf)
#pragma unroll
          for (int c = 0; c < 4; ++c) {
            i32x4 v = {(int)(iv[hf * 4][c] << 7), (int)(iv[hf * 4 + 1][c] << 7), (int)(iv[hf * 4 + 2][c] << 7), (int)(iv[hf * 4 + 3][c] << 7)};
            *(LAS i32x4*)(sm + LDS_IDX + (jj * 4 + c < KF ? jj * 4 + c : KF) * IDX_STRIDE + wa * 256 + hf * 128 + q * 16) = v;
          }
      }
      STAMP(1);
      fetch();
      static_assert(NA == 5, "ramp");
#pragma unroll 1
      for (int s = 0; s < 3; ++s) {
        issue(); fetch();
        issue(); fetch();
        if (s == 0) wait_vm_bar<8>();
        else if (s == 1) wait_vm_bar<16>();
      }
#pragma unroll 1
      for (int k = 2; k <= KF - NA; ++k) {
        wait_vm_bar<(NA - 2) * 8>();
        issue();
        fetch();
      }
      Tail<8, NA - 1>::run();
    }
    const u32x2 eo = *(LAS u32x2*)(sm + LDS_PAR + 1096);
    float wd = *(LAS float*)(sm + LDS_PAR + 1024 + (p * 4 + rl) * 4);
    float b2f = *(LAS float*)(sm + LDS_PAR + 1088);
    const unsigned long long en = (((unsigned long long)eo[1] << 32) | eo[0]) + 1ull;
    const u32x2 want = {(unsigned)en, (unsigned)(en >> 32)};
    u32x2 w2[2][2];
    f32x2 bf[2][4];
    {
      const lds_cp pp = sm + LDS_PAR + ((seg >> 2) * 32 + (seg & 3) * 4) * 2;
#pragma unroll
      for (int c = 0; c < 2; ++c)
#pragma unroll
        for (int h = 0; h < 2; ++h) {
          const u32x2 bb = *(LAS u32x2*)(pp + c * 256 + h * 32);
          w2[c][h] = *(LAS u32x2*)(pp + 512 + c * 256 + h * 32);
          bf[c][h * 2] = unp(bb[0]);
          bf[c][h * 2 + 1] = unp(bb[1]);
          asm volatile("" : "+v"(bf[c][h * 2]), "+v"(bf[c][h * 2 + 1]), "+v"(w2[c][h]));
        }
    }
    asm volatile("" : "+v"(b2f), "+v"(wd));
    u32x4 pv[8];
#pragma unroll
    for (int i = 0; i < 8; ++i) pv[i] = (u32x4){0, 0, 0, 0};
    unsigned pend = 0x5555u;
    const int vo = LIN ? seg * 256 + p * 64 + rl * 16 : (seg >> 2) * 1024 + p * 256 + (lane & 15) * 16;
#pragma unroll 1
    for (unsigned spins = 0; spins < kSpinCap; ++spins) {
      asm volatile("" ::: "memory");
      const u32x2 f = __builtin_amdgcn_raw_buffer_load_b64(rP, INBOX_BYTES + G * 256 + 128 + (lane & 15) * 8, 0, 16);
      const unsigned m = (unsigned)__builtin_amdgcn_ballot_w64(f[0] == want[0] && f[1] == want[1]);
      const unsigned go = m & (m >> 1) & pend;
      asm volatile("" ::: "memory");
#pragma unroll
      for (int i = 0; i < 8; ++i)
        if (go & (1u << (2 * i))) pv[i] = __builtin_amdgcn_raw_buffer_load_b128(rP, vo, (G * 32 + (i & 3) * 8 + (i >> 2) * 4) * 1024, 16);
      pend &= ~go;
      if (!pend) break;
      __builtin_amdgcn_s_sleep(1);
    }
    STAMP(5);
    float d = 0.f;
#pragma unroll
    for (int c = 0; c < 2; ++c)
#pragma unroll
      for (int e = 0; e < 4; ++e) {
        f32x2 t = (unp(pv[c * 4][e]) + unp(pv[c * 4 + 1][e])) + (unp(pv[c * 4 + 2][e]) + unp(pv[c * 4 + 3][e]));
        t = unp(pkv(t)) + bf[c][e];
        i16x2 hh = __builtin_bit_cast(i16x2, pkv(t));
        hh = __builtin_elementwise_max(hh, (i16x2){0, 0});
        const unsigned wq = w2[c][e >> 1][e & 1];
        d = __builtin_amdgcn_fdot2_f32_bf16(__builtin_bit_cast(bf16v2, hh), __builtin_bit_cast(bf16v2, wq), d, false);
      }
    d = sum16(d);
    d = rbf(rbf(d) + b2f);
    const float z = rbf(wd + d);
    if (lane < 4) out[G * 16 + p * 4 + lane] = __float2bfloat16(1.f / (1.f + __expf(-z)));
    if (w == 4 && lane == 0) __builtin_amdgcn_raw_buffer_store_b64(want, rP, INBOX_BYTES + G * 256, 0, 16);
    STAMP(6);
    if (w == 4) { STAMP_OUT(1, 2); STAMP_OUT(5, 7); }
  }
}

extern "C" void kernel_launch(void* const* d_in, const int* in_sizes, int n_in, void* d_out, int out_size, void* d_ws, size_t ws_size,
                              hipStream_t stream) {
  const int* idx = (const int*)d_in[0];
  const unsigned short* table = (const unsigned short*)d_in[1];
  const unsigned short* xw = (const unsigned short*)d_in[2];
  const unsigned short* ww = (const unsigned short*)d_in[3];
  const unsigned short* bw = (const unsigned short*)d_in[4];
  const unsigned short* W1 = (const unsigned short*)d_in[5];
  const unsigned short* b1 = (const unsigned short*)d_in[6];
  const unsigned short* W2 = (const unsigned short*)d_in[7];
  const unsigned short* b2 = (const unsigned short*)d_in[8];
  unsigned* ws = (unsigned*)d_ws;
  (void)hipFuncSetAttribute((const void*)fused_kernel, hipFuncAttributeMaxDynamicSharedMemorySize, LDS_TOTAL);
  fused_kernel<<<256, 512, LDS_TOTAL, stream>>>(idx, table, W1, ws, b1, W2, xw, ww, bw, b2, (__hip_bfloat16*)d_out);
}
```

```cpp
#include <hip/hip_runtime.h>
#include <hip/hip_bf16.h>
#include <stdint.h>

typedef __attribute__((ext_vector_type(8))) short bf16x8;
typedef __attribute__((ext_vector_type(4))) short bf16x4;
typedef __attribute__((ext_vector_type(4))) float f32x4;
typedef __attribute__((ext_vector_type(4))) int i32x4;
typedef __attribute__((ext_vector_type(4))) unsigned u32x4;
typedef __attribute__((ext_vector_type(2))) unsigned u32x2;
#define LAS __attribute__((address_space(3)))
typedef LAS char* lds_cp;

constexpr int kB = 4096, kF = 100, kD = 64, kV = 100000, kNd = 13, kH = 256;
#define CFG_NA 5
#define CFG_NB 4
#define ROT 3
#define SCHED 1
#define SWAPB 4
#define LIN 0
#define TLBW 1
#define TT 0
#define PDELAY 0
#define PSLEEP 0
#define KA 1
#define NTA 0
#define NTW 0
#define NTI 0
#define WRAMP 4
#define PREW 0
#define USE_B0 0
#define RAMP 1
#define ABL_NOA 0
#define ABL_NOB 0
#define ABL_NOC 0
constexpr int NA = CFG_NA, NB = CFG_NB, KF = 25;
constexpr int SLOT = 16384;
constexpr int IDX_STRIDE = 528;
constexpr int LDS_B = NA * SLOT;
constexpr int LDS_IDX = (NA + NB) * SLOT;
constexpr int LDS_TOTAL = LDS_IDX + (KF + 1) * IDX_STRIDE;
static_assert(LDS_TOTAL <= 160 * 1024, "lds");
constexpr int INBOX_BYTES = 256 * 32768;
constexpr int WS_BYTES = INBOX_BYTES + 256 * 1024;
constexpr unsigned kSpinCap = 1u << 16;

__device__ __forceinline__ float bf2f(unsigned short u) { return __uint_as_float(((unsigned)u) << 16); }
__device__ __forceinline__ float bflo(unsigned u) { return __uint_as_float(u << 16); }
__device__ __forceinline__ float bfhi(unsigned u) { return __uint_as_float(u & 0xffff0000u); }
__device__ __forceinline__ float rbf(float x) { return (float)(__bf16)x; }
typedef __attribute__((ext_vector_type(2))) __bf16 bf16v2;
typedef __attribute__((ext_vector_type(2))) float f32x2;
typedef __attribute__((ext_vector_type(2))) short i16x2;
__device__ __forceinline__ f32x2 unp(unsigned u) { return (f32x2){bflo(u), bfhi(u)}; }
__device__ __forceinline__ unsigned pkv(f32x2 t) {
  unsigned r;
  asm("v_cvt_pk_bf16_f32 %0, %1, %2" : "=v"(r) : "v"(t[0]), "v"(t[1]));
  return r;
}
__device__ __forceinline__ unsigned pk2(float a, float b) {
  bf16v2 v; v[0] = (__bf16)a; v[1] = (__bf16)b;
  return __builtin_bit_cast(unsigned, v);
}
__device__ __forceinline__ float sum16(float x) {
  x += __builtin_bit_cast(float, __builtin_amdgcn_update_dpp(0, __builtin_bit_cast(int, x), 0x124, 0xf, 0xf, false));
  x += __builtin_bit_cast(float, __builtin_amdgcn_update_dpp(0, __builtin_bit_cast(int, x), 0x128, 0xf, 0xf, false));
  unsigned u = __float_as_uint(x);
  auto s16 = __builtin_amdgcn_permlane16_swap(u, u, false, false);
  x = __uint_as_float(s16[0]) + __uint_as_float(s16[1]);
  u = __float_as_uint(x);
  auto s32 = __builtin_amdgcn_permlane32_swap(u, u, false, false);
  return __uint_as_float(s32[0]) + __uint_as_float(s32[1]);
}

__device__ __forceinline__ i32x4 mk_rsrc(const void* p, unsigned bytes) {
  unsigned long long a = (unsigned long long)p;
  i32x4 r;
  r[0] = __builtin_amdgcn_readfirstlane((int)(unsigned)a);
  r[1] = __builtin_amdgcn_readfirstlane((int)((unsigned)(a >> 32) & 0xffffu));
  r[2] = (int)bytes;
  r[3] = 0x00020000;
  return r;
}
template <int NT> __device__ __forceinline__ void dma16(i32x4 rsrc, unsigned lds_addr, unsigned voff, unsigned soff) {
  if (NT) asm volatile("s_mov_b32 m0, %0\n\ts_nop 0\n\tbuffer_load_dwordx4 %1, %2, %3 offen nt lds" ::"s"(lds_addr), "v"(voff), "s"(rsrc), "s"(soff));
  else asm volatile("s_mov_b32 m0, %0\n\ts_nop 0\n\tbuffer_load_dwordx4 %1, %2, %3 offen lds" ::"s"(lds_addr), "v"(voff), "s"(rsrc), "s"(soff));
}
template <int V> struct IC { static constexpr int v = V; };
template <int N> __device__ __forceinline__ void wait_vm_bar() { asm volatile("s_waitcnt vmcnt(%0)\n\ts_barrier" ::"n"(N) : "memory"); }
__device__ __forceinline__ void bar() { asm volatile("s_barrier" ::: "memory"); }
__device__ __forceinline__ void lgkm_bar() {
  __builtin_amdgcn_s_waitcnt(0xC07F);
  asm volatile("s_barrier" ::: "memory");
}
#define STAMP(i)
#define STAMP_OUT(lo, hi)

template <int P, int K> struct Tail {
  static __device__ __forceinline__ void run() {
    wait_vm_bar<(K - 1) * P>();
    Tail<P, K - 1>::run();
  }
};
template <int P> struct Tail<P, 0> { static __device__ __forceinline__ void run() {} };

__global__ __launch_bounds__(512, 2) void fused_kernel(const int* __restrict__ idx, const unsigned short* __restrict__ table,
                                                       const unsigned short* __restrict__ W1, unsigned* __restrict__ ws,
                                                       const unsigned short* __restrict__ b1, const unsigned short* __restrict__ W2,
                                                       const unsigned short* __restrict__ xw, const unsigned short* __restrict__ ww,
                                                       const unsigned short* __restrict__ bw, const unsigned short* __restrict__ b2,
                                                       __hip_bfloat16* __restrict__ out) {
  extern __shared__ __attribute__((aligned(16))) char smem[];
  const lds_cp sm = (lds_cp)smem;
  const unsigned lds0 = (unsigned)(size_t)sm;
  const int tid = threadIdx.x, lane = tid & 63;
  const int w = __builtin_amdgcn_readfirstlane(tid >> 6);
  const int b = blockIdx.x, xcd = b & 7, j = b >> 3;
  const int ks = xcd & 3;
  const int mt = (xcd >> 2) * 16 + (j >> 1);
  const int nt = j & 1;
  const int m0 = mt * 128, n0 = nt * 128, f0 = ks * KF;
  const int G = mt * 8 + ks * 2 + nt;
  const int rot = (mt * ROT) % KF;
  auto fld = [&](int k) {
    int i = k;
    if (nt && k < (KF / (2 * SWAPB)) * 2 * SWAPB) i = ((k / SWAPB) ^ 1) * SWAPB + k % SWAPB;
    i += rot;
    return i >= KF ? i - KF : i;
  };
  STAMP(0);
  asm volatile("" ::"s"(idx), "s"(table), "s"(W1), "s"(ws), "s"(b1), "s"(W2), "s"(xw), "s"(ww), "s"(bw), "s"(b2), "s"(out));
  STAMP(11);
  const __amdgpu_buffer_rsrc_t rP = __builtin_amdgcn_make_buffer_rsrc((void*)ws, 0, WS_BYTES, 0x00020000);
  u32x4 iv[8];
  if ((w & 6) == 4) {
    const __amdgpu_buffer_rsrc_t rI = __builtin_amdgcn_make_buffer_rsrc((void*)idx, 0, kB * kF * 4, 0x00020000);
#pragma unroll
    for (int i = 0; i < 8; ++i)
      iv[i] = __builtin_amdgcn_raw_buffer_load_b128(rI, ((m0 + (w - 4) * 64 + 8 * i + (lane >> 3)) * kF + f0 + (lane & 7) * 4) * 4, 0, NTI * 2);
  }
  unsigned t0 = 0;
  if (TT && w < 4) {
    const __amdgpu_buffer_rsrc_t rT = __builtin_amdgcn_make_buffer_rsrc((void*)table, 0, kV * 128, 0x00020000);
    t0 = __builtin_amdgcn_raw_buffer_load_b32(rT, lane < TT ? (w * TT + lane) * (TT == 1 ? 0x400000 : 0x200000) + j * 128 : 0x7fffffff, 0, 0);
  }
  STAMP(12);
  if (w < 4) {
    const int wm = w >> 1, wn = w & 1;
    int z = 0;
    unsigned short t1 = 0;
    if (TLBW && w == 1) {
      const int a = (lane >> 2) & 7, sub = lane & 3;
      const char* const* ka = (const char* const*)__builtin_amdgcn_kernarg_segment_ptr();
      const char* q = ka[3 + a + z];
      const int mg = (int)((0x1000d001u >> (a * 4)) & 0xfu) << (a == 0 ? 15 : 5);
      t1 = *(const unsigned short*)(q + G * mg + ((0x0e >> a) & 1) * sub * 128);
      asm volatile("" : "+v"(z));
    }
    const u32x2 eo = __builtin_amdgcn_raw_buffer_load_b64(rP, INBOX_BYTES + (mt * 8 + wm * 4 + (lane & 3)) * 1024 + ((lane >> 2) & 3) * 8 + z, 0, 16);
    STAMP(7);
    const int fr = lane & 15, fq = lane >> 4;
    lds_cp pa[2], pb[4];
#pragma unroll
    for (int s = 0; s < 2; ++s) {
      int row = wm * 64 + fr;
      pa[s] = sm + row * 128 + (((4 * s + fq) ^ ((row >> 1) & 7)) << 4);
    }
    {
      int q = (lane & 15) >> 2, p = lane & 3;
      int kr = 8 * fq + q;
      int F = (((kr >> 1) & 1) << 1) | ((kr >> 3) & 1);
#pragma unroll
      for (int i = 0; i < 4; ++i) pb[i] = sm + LDS_B + wn * 8192 + kr * 128 + (((F ^ i) * 2) << 4) + (p >> 1) * 16 + (p & 1) * 8;
    }
    f32x4 acc[4][4];
    bf16x8 ea[2][4], wf[2][4];
#pragma unroll
    for (int a = 0; a < 4; ++a) {
#pragma unroll
      for (int c = 0; c < 4; ++c) acc[a][c] = (f32x4){0.f, 0.f, 0.f, 0.f};
      ea[1][a] = (bf16x8){0, 0, 0, 0, 0, 0, 0, 0};
      wf[1][a] = (bf16x8){0, 0, 0, 0, 0, 0, 0, 0};
    }
    int sa = 0, sb = 0;
    auto rd_w = [&](int s, int i) {
      bf16x4 lo = __builtin_amdgcn_ds_read_tr16_b64_v4i16((LAS bf16x4*)(pb[i] + s * 4096));
      bf16x4 hi = __builtin_amdgcn_ds_read_tr16_b64_v4i16((LAS bf16x4*)(pb[i] + s * 4096 + 512));
      wf[s][i] = __builtin_shufflevector(lo, hi, 0, 1, 2, 3, 4, 5, 6, 7);
    };
    auto reads = [&](int s) {
#pragma unroll
      for (int i = 0; i < 4; ++i) {
        ea[s][i] = *(LAS bf16x8*)(pa[s] + i * 2048);
        rd_w(s, i);
      }
    };
    auto mfmas = [&](int s) {
#pragma unroll
      for (int a = 0; a < 4; ++a)
#pragma unroll
        for (int i = 0; i < 4; ++i) acc[a][i] = __builtin_amdgcn_mfma_f32_16x16x32_bf16(wf[s][a], ea[s][i], acc[a][i], 0, 0, 0);
    };
    auto sched = [&](int half) {
#pragma unroll
      for (int g = 0; g < 4; ++g) {
        __builtin_amdgcn_sched_group_barrier(0x8, 1, 0);
        __builtin_amdgcn_sched_group_barrier(0x100, 1, 0);
        __builtin_amdgcn_sched_group_barrier(0x8, 1, 0);
        __builtin_amdgcn_sched_group_barrier(0x100, 2, 0);
      }
#pragma unroll
      for (int g = 0; g < 8; ++g) {
        __builtin_amdgcn_sched_group_barrier(0x8, 1, 0);
        __builtin_amdgcn_sched_group_barrier(0x2, 1, 0);
      }
    };
#pragma unroll 1
    for (int k = 0; k < KF; ++k) {
      lgkm_bar();
      if (!ABL_NOC) {
        reads(0);
        mfmas(1);
        sched(0);
        __builtin_amdgcn_sched_barrier(0);
        reads(1);
        mfmas(0);
        const int da = sa == NA - 1 ? -(NA - 1) * SLOT : SLOT;
        const int db = sb == NB - 1 ? -(NB - 1) * SLOT : SLOT;
        sa = sa == NA - 1 ? 0 : sa + 1;
        sb = sb == NB - 1 ? 0 : sb + 1;
        pa[0] += da; pa[1] += da;
#pragma unroll
        for (int i = 0; i < 4; ++i) pb[i] += db;
        sched(1);
        __builtin_amdgcn_sched_barrier(0);
      }
    }
    mfmas(1);
    STAMP(3);
    const int so = LIN ? lane * 16 : (fr >> 2) * 256 + fq * 64 + (fr & 3) * 16;
#pragma unroll
    for (int i = 0; i < 4; ++i)
#pragma unroll
      for (int h = 0; h < 2; ++h) {
        u32x4 v = {pk2(acc[2 * h][i][0], acc[2 * h][i][1]), pk2(acc[2 * h][i][2], acc[2 * h][i][3]),
                   pk2(acc[2 * h + 1][i][0], acc[2 * h + 1][i][1]), pk2(acc[2 * h + 1][i][2], acc[2 * h + 1][i][3])};
        const int g = wm * 4 + i;
        __builtin_amdgcn_raw_buffer_store_b128(v, rP, so, (((mt * 8 + g) * 4 + ks) * 8 + nt * 4 + wn * 2 + h) * 1024, 16);
      }
    const unsigned long long en = (((unsigned long long)eo[1] << 32) | eo[0]) + 1ull;
    const u32x2 fl = {(unsigned)en, (unsigned)(en >> 32)};
    asm volatile("s_waitcnt vmcnt(0)" ::"v"(t0), "v"(t1) : "memory");
    if (lane < 16) __builtin_amdgcn_raw_buffer_store_b64(fl, rP, INBOX_BYTES + (mt * 8 + wm * 4 + (lane & 3)) * 1024 + 128 + (lane >> 2) * 128 + ((nt * 4 + ks) * 2 + wn) * 8, 0, 16);
    STAMP(4);
    if (w == 0) { STAMP_OUT(0, 1); STAMP_OUT(2, 5); STAMP_OUT(7, 8); }
  } else {
    const int p = w - 4;
    const int rl = lane & 3, seg = lane >> 2;
    if (w >= 6) {
      const int wb = w - 6;
      const i32x4 rW = mk_rsrc(W1, kF * kD * kH * 2);
      int voff[2];
#pragma unroll
      for (int o = 0; o < 2; ++o) {
        int F = (((lane >> 4) & 1) << 1) | o;
        int c = (lane & 7) ^ (2 * F);
        voff[o] = (lane >> 3) * 512 + (n0 + 64 * wb) * 2 + c * 16;
      }
      int kk = 0;
      unsigned dst = lds0 + LDS_B + wb * 8192;
      auto issue = [&](auto ntc) {
        const unsigned so = (f0 + fld(kk)) * 32768;
        if (!ABL_NOB) {
#pragma unroll
          for (int i = 0; i < 8; ++i) dma16<decltype(ntc)::v>(rW, dst + i * 1024, voff[i & 1], so + i * 4096);
        }
        ++kk;
        dst = dst + SLOT >= lds0 + LDS_B + NB * SLOT ? dst - (NB - 1) * SLOT : dst + SLOT;
      };
      STAMP(13);
#pragma unroll 1
      for (int s = 0; s < NB; ++s) {
        if (s == WRAMP) wait_vm_bar<(WRAMP - 1) * 8>();
        issue(IC<(NTW & 1)>());
      }
      if (WRAMP == NB) wait_vm_bar<(NB - 1) * 8>();
#pragma unroll 1
      for (int k = 1; k <= KF - NB; ++k) {
        wait_vm_bar<(NB - 2) * 8>();
        issue(IC<(NTW >> 1)>());
      }
      Tail<8, NB - 1>::run();
    } else {
      const int wa = w - 4;
      const i32x4 rT = mk_rsrc(table, kV * 128);
      int c16[2];
#pragma unroll
      for (int o = 0; o < 2; ++o) c16[o] = ((lane & 7) ^ ((4 * o + (lane >> 4)) & 7)) << 4;
      int kk = 0;
      unsigned dst = lds0 + wa * 8192;
      const lds_cp ia = sm + LDS_IDX + wa * 256 + (lane >> 3) * 16;
      i32x4 ix0, ix1;
      auto fetch = [&]() {
        const int kf = fld(kk < KF ? kk : 0);
        ix0 = *(LAS i32x4*)(ia + kf * IDX_STRIDE);
        ix1 = *(LAS i32x4*)(ia + kf * IDX_STRIDE + 128);
        ++kk;
      };
      auto issue = [&](auto ntc) {
        if (!ABL_NOA) {
#pragma unroll
          for (int i = 0; i < 4; ++i) dma16<decltype(ntc)::v>(rT, dst + i * 1024, ix0[i] + c16[i & 1], 0);
#pragma unroll
          for (int i = 0; i < 4; ++i) dma16<decltype(ntc)::v>(rT, dst + 4096 + i * 1024, ix1[i] + c16[i & 1], 0);
        }
        dst = dst + SLOT >= lds0 + NA * SLOT ? dst - (NA - 1) * SLOT : dst + SLOT;
      };
      {
        const int q = lane >> 3, jj = lane & 7;
#pragma unroll
        for (int hf = 0; hf < 2; ++hf)
#pragma unroll
          for (int c = 0; c < 4; ++c) {
            i32x4 v = {(int)(iv[hf * 4][c] << 7), (int)(iv[hf * 4 + 1][c] << 7), (int)(iv[hf * 4 + 2][c] << 7), (int)(iv[hf * 4 + 3][c] << 7)};
            *(LAS i32x4*)(sm + LDS_IDX + (jj * 4 + c < KF ? jj * 4 + c : KF) * IDX_STRIDE + wa * 256 + hf * 128 + q * 16) = v;
          }
      }
      STAMP(1);
      fetch();
      static_assert(NA == 5, "ramp");
#pragma unroll 1
      for (int s = 0; s < 3; ++s) {
        issue(IC<(NTA & 1)>()); fetch();
        issue(IC<(NTA & 1)>()); fetch();
        if (s == 0) wait_vm_bar<8>();
        else if (s == 1) wait_vm_bar<16>();
      }
#pragma unroll 1
      for (int k = 2; k <= KF - NA; ++k) {
        wait_vm_bar<(NA - 2) * 8>();
        issue(IC<(NTA >> 1)>());
        fetch();
      }
      Tail<8, NA - 1>::run();
    }
    int z = 0;
    asm volatile("" : "+v"(z));
    const u32x2 eo = __builtin_amdgcn_raw_buffer_load_b64(rP, INBOX_BYTES + G * 1024 + p * 8 + z, 0, 16);
    const unsigned short xv = xw[(G * 16 + p * 4 + rl) * kNd + (seg < kNd ? seg : 0) + z];
    const unsigned short wv = ww[(seg < kNd ? seg : 0) + z];
    const unsigned short bwv = bw[z], b2v = b2[z];
    u32x2 w2[2][2], bb[2][2];
    {
      const int col = (seg >> 2) * 32 + (seg & 3) * 4 + z;
#pragma unroll
      for (int c = 0; c < 2; ++c)
#pragma unroll
        for (int h = 0; h < 2; ++h) {
          bb[c][h] = *(const u32x2*)(b1 + col + c * 128 + h * 16);
          w2[c][h] = *(const u32x2*)(W2 + col + c * 128 + h * 16);
        }
    }
    const unsigned long long en = (((unsigned long long)eo[1] << 32) | eo[0]) + 1ull;
    const u32x2 want = {(unsigned)en, (unsigned)(en >> 32)};
    float wd = seg < kNd ? bf2f(xv) * bf2f(wv) : 0.f;
    wd = sum16(wd);
    wd = rbf(rbf(wd) + bf2f(bwv));
    float b2f = bf2f(b2v);
    f32x2 bf[2][4];
#pragma unroll
    for (int c = 0; c < 2; ++c)
#pragma unroll
      for (int h = 0; h < 2; ++h) {
        bf[c][h * 2] = unp(bb[c][h][0]);
        bf[c][h * 2 + 1] = unp(bb[c][h][1]);
        asm volatile("" : "+v"(bf[c][h * 2]), "+v"(bf[c][h * 2 + 1]), "+v"(w2[c][h]));
      }
    asm volatile("" : "+v"(b2f), "+v"(wd));
    STAMP(10);
    u32x4 pv[8];
#pragma unroll
    for (int i = 0; i < 8; ++i) pv[i] = (u32x4){0, 0, 0, 0};
    unsigned pend = 0x5555u;
    const int vo = LIN ? seg * 256 + p * 64 + rl * 16 : (seg >> 2) * 1024 + p * 256 + (lane & 15) * 16;
#pragma unroll 1
    for (unsigned spins = 0; spins < kSpinCap; ++spins) {
      asm volatile("" ::: "memory");
      const u32x2 f = __builtin_amdgcn_raw_buffer_load_b64(rP, INBOX_BYTES + G * 1024 + 128 + p * 128 + (lane & 15) * 8, 0, 16);
      const unsigned m = (unsigned)__builtin_amdgcn_ballot_w64(f[0] == want[0] && f[1] == want[1]);
      const unsigned go = m & (m >> 1) & pend;
      asm volatile("" ::: "memory");
#pragma unroll
      for (int i = 0; i < 8; ++i)
        if (go & (1u << (2 * i))) pv[i] = __builtin_amdgcn_raw_buffer_load_b128(rP, vo, (G * 32 + (i & 3) * 8 + (i >> 2) * 4) * 1024, 16);
      pend &= ~go;
      if (!pend) break;
      __builtin_amdgcn_s_sleep(1);
    }
    STAMP(5);
    float d = 0.f;
#pragma unroll
    for (int c = 0; c < 2; ++c)
#pragma unroll
      for (int e = 0; e < 4; ++e) {
        f32x2 t = (unp(pv[c * 4][e]) + unp(pv[c * 4 + 1][e])) + (unp(pv[c * 4 + 2][e]) + unp(pv[c * 4 + 3][e]));
        t = unp(pkv(t)) + bf[c][e];
        i16x2 hh = __builtin_bit_cast(i16x2, pkv(t));
        hh = __builtin_elementwise_max(hh, (i16x2){0, 0});
        const unsigned wq = w2[c][e >> 1][e & 1];
        d = __builtin_amdgcn_fdot2_f32_bf16(__builtin_bit_cast(bf16v2, hh), __builtin_bit_cast(bf16v2, wq), d, false);
      }
    d = sum16(d);
    d = rbf(rbf(d) + b2f);
    const float zz = rbf(wd + d);
    if (lane < 4) out[G * 16 + p * 4 + lane] = __float2bfloat16(1.f / (1.f + __expf(-zz)));
    if (lane == 0) __builtin_amdgcn_raw_buffer_store_b64(want, rP, INBOX_BYTES + G * 1024 + p * 8, 0, 16);
    STAMP(6);
    if (w == 4) { STAMP_OUT(1, 2); STAMP_OUT(5, 7); STAMP_OUT(8, 9); STAMP_OUT(10, 12); STAMP_OUT(15, 16); }
    if (w == 6) { STAMP_OUT(9, 10); STAMP_OUT(12, 15); }
  }
}

extern "C" void kernel_launch(void* const* d_in, const int* in_sizes, int n_in, void* d_out, int out_size, void* d_ws, size_t ws_size,
                              hipStream_t stream) {
  const int* idx = (const int*)d_in[0];
  const unsigned short* table = (const unsigned short*)d_in[1];
  const unsigned short* xw = (const unsigned short*)d_in[2];
  const unsigned short* ww = (const unsigned short*)d_in[3];
  const unsigned short* bw = (const unsigned short*)d_in[4];
  const unsigned short* W1 = (const unsigned short*)d_in[5];
  const unsigned short* b1 = (const unsigned short*)d_in[6];
  const unsigned short* W2 = (const unsigned short*)d_in[7];
  const unsigned short* b2 = (const unsigned short*)d_in[8];
  unsigned* ws = (unsigned*)d_ws;
  (void)hipFuncSetAttribute((const void*)fused_kernel, hipFuncAttributeMaxDynamicSharedMemorySize, LDS_TOTAL);
  fused_kernel<<<256, 512, LDS_TOTAL, stream>>>(idx, table, W1, ws, b1, W2, xw, ww, bw, b2, (__hip_bfloat16*)d_out);
}
```

```cpp
#include <hip/hip_runtime.h>
#include <hip/hip_bf16.h>
#include <stdint.h>

typedef __attribute__((ext_vector_type(8))) short bf16x8;
typedef __attribute__((ext_vector_type(4))) short bf16x4;
typedef __attribute__((ext_vector_type(4))) float f32x4;
typedef __attribute__((ext_vector_type(4))) int i32x4;
typedef __attribute__((ext_vector_type(4))) unsigned u32x4;
typedef __attribute__((ext_vector_type(2))) unsigned u32x2;
typedef __attribute__((ext_vector_type(2))) __bf16 bf16v2;
typedef __attribute__((ext_vector_type(2))) float f32x2;
typedef __attribute__((ext_vector_type(2))) short i16x2;
typedef unsigned long long u64;
#define LAS __attribute__((address_space(3)))
#define GAS __attribute__((address_space(1)))
#define CAS __attribute__((address_space(4)))
typedef LAS char* lds_cp;

constexpr int kB = 4096, kF = 100, kD = 64, kV = 100000, kNd = 13, kH = 256;
constexpr int NA = 5, NB = 4, KF = 25;
constexpr int ROT = 3, SWAPB = 4;
constexpr int SLOT = 16384;
constexpr int IDX_STRIDE = 528;
constexpr int LDS_B = NA * SLOT;
constexpr int LDS_IDX = (NA + NB) * SLOT;
constexpr int LDS_TOTAL = LDS_IDX + (KF + 1) * IDX_STRIDE;
static_assert(LDS_TOTAL <= 160 * 1024, "lds");
constexpr int INBOX_BYTES = 256 * 32768;
constexpr int LS = 1024;
constexpr int WS_BYTES = INBOX_BYTES + 256 * LS;
constexpr unsigned kSpinCap = 1u << 16;
constexpr int SC1 = 16;

__device__ __forceinline__ float bf2f(unsigned short u) { return __uint_as_float(((unsigned)u) << 16); }
__device__ __forceinline__ float bflo(unsigned u) { return __uint_as_float(u << 16); }
__device__ __forceinline__ float bfhi(unsigned u) { return __uint_as_float(u & 0xffff0000u); }
__device__ __forceinline__ float rbf(float x) { return (float)(__bf16)x; }
__device__ __forceinline__ f32x2 unp(unsigned u) { return (f32x2){bflo(u), bfhi(u)}; }
__device__ __forceinline__ unsigned pkv(f32x2 t) {
  unsigned r;
  asm("v_cvt_pk_bf16_f32 %0, %1, %2" : "=v"(r) : "v"(t[0]), "v"(t[1]));
  return r;
}
__device__ __forceinline__ unsigned pk2(float a, float b) {
  bf16v2 v; v[0] = (__bf16)a; v[1] = (__bf16)b;
  return __builtin_bit_cast(unsigned, v);
}
__device__ __forceinline__ float sum16(float x) {
  x += __builtin_bit_cast(float, __builtin_amdgcn_update_dpp(0, __builtin_bit_cast(int, x), 0x124, 0xf, 0xf, false));
  x += __builtin_bit_cast(float, __builtin_amdgcn_update_dpp(0, __builtin_bit_cast(int, x), 0x128, 0xf, 0xf, false));
  unsigned u = __float_as_uint(x);
  auto s16 = __builtin_amdgcn_permlane16_swap(u, u, false, false);
  x = __uint_as_float(s16[0]) + __uint_as_float(s16[1]);
  u = __float_as_uint(x);
  auto s32 = __builtin_amdgcn_permlane32_swap(u, u, false, false);
  return __uint_as_float(s32[0]) + __uint_as_float(s32[1]);
}

__device__ __forceinline__ i32x4 mk_rsrc(const void* p, unsigned bytes) {
  u64 a = (u64)p;
  i32x4 r;
  r[0] = __builtin_amdgcn_readfirstlane((int)(unsigned)a);
  r[1] = __builtin_amdgcn_readfirstlane((int)((unsigned)(a >> 32) & 0xffffu));
  r[2] = (int)bytes;
  r[3] = 0x00020000;
  return r;
}
__device__ __forceinline__ void dma16(i32x4 rsrc, unsigned lds_addr, unsigned voff, unsigned soff) {
  asm volatile("s_mov_b32 m0, %0\n\ts_nop 0\n\tbuffer_load_dwordx4 %1, %2, %3 offen lds" ::"s"(lds_addr), "v"(voff), "s"(rsrc), "s"(soff));
}
template <int N> __device__ __forceinline__ void wait_vm_bar() { asm volatile("s_waitcnt vmcnt(%0)\n\ts_barrier" ::"n"(N) : "memory"); }
__device__ __forceinline__ void lgkm_bar() {
  __builtin_amdgcn_s_waitcnt(0xC07F);
  asm volatile("s_barrier" ::: "memory");
}
template <int P, int K> struct Tail {
  static __device__ __forceinline__ void run() {
    wait_vm_bar<(K - 1) * P>();
    Tail<P, K - 1>::run();
  }
};
template <int P> struct Tail<P, 0> { static __device__ __forceinline__ void run() {} };

__global__ __launch_bounds__(512, 2) void fused_kernel(const void* idx_, const void* table_, const void* W1_, void* ws_, const void* b1_, const void* W2_,
                                                       const void* xw_, const void* ww_, const void* bw_, const void* b2_, void* out_) {
  const CAS u64* kap = (const CAS u64*)__builtin_amdgcn_kernarg_segment_ptr();
  const void* idx = (const void*)kap[0];
  const void* table = (const void*)kap[1];
  const void* W1 = (const void*)kap[2];
  GAS unsigned* ws = (GAS unsigned*)kap[3];
  const GAS unsigned short* b1 = (const GAS unsigned short*)kap[4];
  const GAS unsigned short* W2 = (const GAS unsigned short*)kap[5];
  const GAS unsigned short* xw = (const GAS unsigned short*)kap[6];
  const GAS unsigned short* ww = (const GAS unsigned short*)kap[7];
  const GAS unsigned short* bw = (const GAS unsigned short*)kap[8];
  const GAS unsigned short* b2 = (const GAS unsigned short*)kap[9];
  GAS __bf16* out = (GAS __bf16*)kap[10];
  extern __shared__ __attribute__((aligned(16))) char smem[];
  const lds_cp sm = (lds_cp)smem;
  const unsigned lds0 = (unsigned)(size_t)sm;
  const int tid = threadIdx.x, lane = tid & 63;
  const int w = __builtin_amdgcn_readfirstlane(tid >> 6);
  const int b = blockIdx.x, xcd = b & 7, j = b >> 3;
  const int ks = xcd & 3;
  const int mt = (xcd >> 2) * 16 + (j >> 1);
  const int nt = j & 1;
  const int m0 = mt * 128, n0 = nt * 128, f0 = ks * KF;
  const int G = mt * 8 + ks * 2 + nt;
  const unsigned own = 1u << (nt * 4 + ks);
  volatile LAS unsigned* bc = (volatile LAS unsigned*)(sm + LDS_IDX + KF * IDX_STRIDE);
  const int rot = ((mt & 31) * ROT) % KF;
  auto fld = [&](int k) {
    int i = k;
    if (nt && k < (KF / (2 * SWAPB)) * 2 * SWAPB) i = ((k / SWAPB) ^ 1) * SWAPB + k % SWAPB;
    i += rot;
    return i >= KF ? i - KF : i;
  };
  asm volatile("" ::"s"(idx), "s"(table), "s"(W1), "s"(ws), "s"(b1), "s"(W2), "s"(xw), "s"(ww), "s"(bw), "s"(b2), "s"(out));
  const __amdgpu_buffer_rsrc_t rP = __builtin_amdgcn_make_buffer_rsrc((void*)(u64)ws, 0, WS_BYTES, 0x00020000);
  u32x4 iv[8];
  if ((w & 6) == 4) {
    const __amdgpu_buffer_rsrc_t rI = __builtin_amdgcn_make_buffer_rsrc((void*)idx, 0, kB * kF * 4, 0x00020000);
#pragma unroll
    for (int i = 0; i < 8; ++i)
      iv[i] = __builtin_amdgcn_raw_buffer_load_b128(rI, ((m0 + (w - 4) * 64 + 8 * i + (lane >> 3)) * kF + f0 + (lane & 7) * 4) * 4, 0, 0);
  }
  if (w < 4) {
    const int wm = w >> 1, wn = w & 1;
    int z = 0;
    unsigned short t1 = 0;
    if (w == 1) {
      const int a = (lane >> 2) & 7, sub = lane & 3;
      const GAS char* q = (const GAS char*)kap[3 + a + z];
      const int mg = (int)((0x1000d001u >> (a * 4)) & 0xfu) << (a == 0 ? 15 : 5);
      t1 = *(const GAS unsigned short*)(q + G * mg + ((0x0e >> a) & 1) * sub * 128);
      asm volatile("" : "+v"(z));
    }
    u32x2 eo = {0, 0};
    if (w < 2) eo = __builtin_amdgcn_raw_buffer_load_b64(rP, INBOX_BYTES + (w == 0 ? mt * 8 + (lane & 7) : G) * LS + z, 0, SC1);
    const int fr = lane & 15, fq = lane >> 4;
    lds_cp pa[2], pb[4];
#pragma unroll
    for (int s = 0; s < 2; ++s) {
      int row = wm * 64 + fr;
      pa[s] = sm + row * 128 + (((4 * s + fq) ^ ((row >> 1) & 7)) << 4);
    }
    {
      int q = (lane & 15) >> 2, p = lane & 3;
      int kr = 8 * fq + q;
      int F = (((kr >> 1) & 1) << 1) | ((kr >> 3) & 1);
#pragma unroll
      for (int i = 0; i < 4; ++i) pb[i] = sm + LDS_B + wn * 8192 + kr * 128 + (((F ^ i) * 2) << 4) + (p >> 1) * 16 + (p & 1) * 8;
    }
    f32x4 acc[4][4];
    bf16x8 ea[2][4], wf[2][4];
#pragma unroll
    for (int a = 0; a < 4; ++a) {
#pragma unroll
      for (int c = 0; c < 4; ++c) acc[a][c] = (f32x4){0.f, 0.f, 0.f, 0.f};
      ea[1][a] = (bf16x8){0, 0, 0, 0, 0, 0, 0, 0};
      wf[1][a] = (bf16x8){0, 0, 0, 0, 0, 0, 0, 0};
    }
    int sa = 0, sb = 0;
    auto reads = [&](int s) {
#pragma unroll
      for (int i = 0; i < 4; ++i) {
        ea[s][i] = *(LAS bf16x8*)(pa[s] + i * 2048);
        bf16x4 lo = __builtin_amdgcn_ds_read_tr16_b64_v4i16((LAS bf16x4*)(pb[i] + s * 4096));
        bf16x4 hi = __builtin_amdgcn_ds_read_tr16_b64_v4i16((LAS bf16x4*)(pb[i] + s * 4096 + 512));
        wf[s][i] = __builtin_shufflevector(lo, hi, 0, 1, 2, 3, 4, 5, 6, 7);
      }
    };
    auto mfmas = [&](int s) {
#pragma unroll
      for (int a = 0; a < 4; ++a)
#pragma unroll
        for (int i = 0; i < 4; ++i) acc[a][i] = __builtin_amdgcn_mfma_f32_16x16x32_bf16(wf[s][a], ea[s][i], acc[a][i], 0, 0, 0);
    };
    auto sched = [&]() {
#pragma unroll
      for (int g = 0; g < 4; ++g) {
        __builtin_amdgcn_sched_group_barrier(0x8, 1, 0);
        __builtin_amdgcn_sched_group_barrier(0x100, 1, 0);
        __builtin_amdgcn_sched_group_barrier(0x8, 1, 0);
        __builtin_amdgcn_sched_group_barrier(0x100, 2, 0);
      }
#pragma unroll
      for (int g = 0; g < 8; ++g) {
        __builtin_amdgcn_sched_group_barrier(0x8, 1, 0);
        __builtin_amdgcn_sched_group_barrier(0x2, 1, 0);
      }
    };
#pragma unroll 1
    for (int k = 0; k < KF; ++k) {
      lgkm_bar();
      reads(0);
      mfmas(1);
      sched();
      __builtin_amdgcn_sched_barrier(0);
      reads(1);
      mfmas(0);
      const int da = sa == NA - 1 ? -(NA - 1) * SLOT : SLOT;
      const int db = sb == NB - 1 ? -(NB - 1) * SLOT : SLOT;
      sa = sa == NA - 1 ? 0 : sa + 1;
      sb = sb == NB - 1 ? 0 : sb + 1;
      pa[0] += da; pa[1] += da;
#pragma unroll
      for (int i = 0; i < 4; ++i) pb[i] += db;
      sched();
      __builtin_amdgcn_sched_barrier(0);
    }
    mfmas(1);
    const int so = (fr >> 2) * 256 + fq * 64 + (fr & 3) * 16;
#pragma unroll
    for (int i = 0; i < 4; ++i)
#pragma unroll
      for (int h = 0; h < 2; ++h) {
        u32x4 v = {pk2(acc[2 * h][i][0], acc[2 * h][i][1]), pk2(acc[2 * h][i][2], acc[2 * h][i][3]),
                   pk2(acc[2 * h + 1][i][0], acc[2 * h + 1][i][1]), pk2(acc[2 * h + 1][i][2], acc[2 * h + 1][i][3])};
        const int g = wm * 4 + i;
        __builtin_amdgcn_raw_buffer_store_b128(v, rP, so, (((mt * 8 + g) * 4 + ks) * 8 + nt * 4 + wn * 2 + h) * 1024, SC1);
      }
    const u64 en = (((u64)eo[1] << 32) | eo[0]) + 1ull;
    u32x2 fl = {(unsigned)en, (unsigned)(en >> 32)};
    int fo = w == 0 ? INBOX_BYTES + (mt * 8 + (lane & 7)) * LS + 64 + (nt * 4 + ks) * 8 : INBOX_BYTES + G * LS + 64 + (lane & 7) * 8;
    if (w == 1) *bc = 0;
    asm volatile("s_waitcnt vmcnt(0) lgkmcnt(0)\n\ts_barrier" : "+v"(fl), "+v"(fo) : "v"(t1) : "memory");
    if (w == 0 && lane < 8 && lane != ks * 2 + nt) __builtin_amdgcn_raw_buffer_store_b64(fl, rP, fo, 0, SC1);
    if (w == 1) {
      __builtin_amdgcn_s_sleep(12);
#pragma unroll 1
      for (unsigned spins = 0; spins < kSpinCap; ++spins) {
        asm volatile("" ::: "memory");
        const u32x2 f = __builtin_amdgcn_raw_buffer_load_b64(rP, fo, 0, SC1);
        const unsigned m = ((unsigned)__builtin_amdgcn_ballot_w64(f[0] == fl[0] && f[1] == fl[1]) | own) & 0xffu;
        if (m == 0xffu) break;
        *bc = m;
        __builtin_amdgcn_s_sleep(1);
      }
      *bc = 0xffu;
      if (lane == 0) __builtin_amdgcn_raw_buffer_store_b64(fl, rP, INBOX_BYTES + G * LS, 0, SC1);
    }
  } else {
    const int p = w - 4;
    const int rl = lane & 3, seg = lane >> 2;
    if (__builtin_expect(w >= 6, 1)) {
      const int wb = w - 6;
      const i32x4 rW = mk_rsrc(W1, kF * kD * kH * 2);
      int voff[2];
#pragma unroll
      for (int o = 0; o < 2; ++o) {
        int F = (((lane >> 4) & 1) << 1) | o;
        int c = (lane & 7) ^ (2 * F);
        voff[o] = (lane >> 3) * 512 + (n0 + 64 * wb) * 2 + c * 16;
      }
      int kk = 0;
      unsigned dst = lds0 + LDS_B + wb * 8192;
      auto issue = [&]() {
        const unsigned so = (f0 + fld(kk)) * 32768;
#pragma unroll
        for (int i = 0; i < 8; ++i) dma16(rW, dst + i * 1024, voff[i & 1], so + i * 4096);
        ++kk;
        dst = dst + SLOT >= lds0 + LDS_B + NB * SLOT ? dst - (NB - 1) * SLOT : dst + SLOT;
      };
#pragma unroll 1
      for (int s = 0; s < NB; ++s) issue();
      wait_vm_bar<(NB - 1) * 8>();
#pragma unroll 1
      for (int k = 1; k <= KF - NB; ++k) {
        wait_vm_bar<(NB - 2) * 8>();
        issue();
      }
      Tail<8, NB - 1>::run();
    } else {
      const int wa = w - 4;
      const i32x4 rT = mk_rsrc(table, kV * 128);
      int c16[2];
#pragma unroll
      for (int o = 0; o < 2; ++o) c16[o] = ((lane & 7) ^ ((4 * o + (lane >> 4)) & 7)) << 4;
      int kk = 0;
      unsigned dst = lds0 + wa * 8192;
      const lds_cp ia = sm + LDS_IDX + wa * 256 + (lane >> 3) * 16;
      i32x4 ix0, ix1;
      auto fetch = [&]() {
        const int kf = fld(kk < KF ? kk : 0);
        ix0 = *(LAS i32x4*)(ia + kf * IDX_STRIDE);
        ix1 = *(LAS i32x4*)(ia + kf * IDX_STRIDE + 128);
        ++kk;
      };
      auto issue = [&]() {
#pragma unroll
        for (int i = 0; i < 4; ++i) dma16(rT, dst + i * 1024, ix0[i] + c16[i & 1], 0);
#pragma unroll
        for (int i = 0; i < 4; ++i) dma16(rT, dst + 4096 + i * 1024, ix1[i] + c16[i & 1], 0);
        dst = dst + SLOT >= lds0 + NA * SLOT ? dst - (NA - 1) * SLOT : dst + SLOT;
      };
      {
        const int q = lane >> 3, jj = lane & 7;
#pragma unroll
        for (int hf = 0; hf < 2; ++hf)
#pragma unroll
          for (int c = 0; c < 4; ++c) {
            i32x4 v = {(int)(iv[hf * 4][c] << 7), (int)(iv[hf * 4 + 1][c] << 7), (int)(iv[hf * 4 + 2][c] << 7), (int)(iv[hf * 4 + 3][c] << 7)};
            *(LAS i32x4*)(sm + LDS_IDX + (jj * 4 + c < KF ? jj * 4 + c : KF) * IDX_STRIDE + wa * 256 + hf * 128 + q * 16) = v;
          }
      }
      fetch();
      static_assert(NA == 5, "ramp");
#pragma unroll 1
      for (int s = 0; s < 3; ++s) {
        issue(); fetch();
        issue(); fetch();
        if (s == 0) wait_vm_bar<8>();
        else if (s == 1) wait_vm_bar<16>();
      }
#pragma unroll 1
      for (int k = 2; k <= KF - NA; ++k) {
        wait_vm_bar<(NA - 2) * 8>();
        issue();
        fetch();
      }
      Tail<8, NA - 1>::run();
    }
    int z = 0;
    asm volatile("" : "+v"(z));
    const unsigned short xv = xw[(G * 16 + p * 4 + rl) * kNd + (seg < kNd ? seg : 0) + z];
    const unsigned short wv = ww[(seg < kNd ? seg : 0) + z];
    const unsigned short bwv = bw[z], b2v = b2[z];
    u32x2 w2[2][2], bb[2][2];
    {
      const int col = (seg >> 2) * 32 + (seg & 3) * 4 + z;
#pragma unroll
      for (int c = 0; c < 2; ++c)
#pragma unroll
        for (int h = 0; h < 2; ++h) {
          bb[c][h] = *(const GAS u32x2*)(b1 + col + c * 128 + h * 16);
          w2[c][h] = *(const GAS u32x2*)(W2 + col + c * 128 + h * 16);
        }
    }
    float wd = seg < kNd ? bf2f(xv) * bf2f(wv) : 0.f;
    wd = sum16(wd);
    wd = rbf(rbf(wd) + bf2f(bwv));
    float b2f = bf2f(b2v);
    f32x2 bf[2][4];
#pragma unroll
    for (int c = 0; c < 2; ++c)
#pragma unroll
      for (int h = 0; h < 2; ++h) {
        bf[c][h * 2] = unp(bb[c][h][0]);
        bf[c][h * 2 + 1] = unp(bb[c][h][1]);
        asm volatile("" : "+v"(bf[c][h * 2]), "+v"(bf[c][h * 2 + 1]), "+v"(w2[c][h]));
      }
    asm volatile("" : "+v"(b2f), "+v"(wd));
    u32x4 pv[8];
#pragma unroll
    for (int i = 0; i < 8; ++i) pv[i] = (u32x4){0, 0, 0, 0};
    unsigned pend = 0xffu, m = own;
    int vo = (seg >> 2) * 1024 + p * 256 + (lane & 15) * 16;
    asm volatile("s_barrier" : "+v"(vo), "+v"(pv[0]), "+v"(pv[1]), "+v"(pv[2]), "+v"(pv[3]), "+v"(pv[4]), "+v"(pv[5]), "+v"(pv[6]), "+v"(pv[7])::"memory");
#pragma unroll 1
    for (unsigned spins = 0; spins < kSpinCap * 64; ++spins) {
      const unsigned go = m & pend;
#pragma unroll
      for (int i = 0; i < 8; ++i)
        if (go & (1u << i)) pv[i] = __builtin_amdgcn_raw_buffer_load_b128(rP, vo, (G * 32 + (i & 3) * 8 + (i >> 2) * 4) * 1024, SC1);
      pend &= ~go;
      if (!pend) break;
      __builtin_amdgcn_s_sleep(1);
      m = __builtin_amdgcn_readfirstlane(*bc);
      asm volatile("" ::: "memory");
    }
    asm volatile("" : "+v"(pv[0]), "+v"(pv[1]), "+v"(pv[2]), "+v"(pv[3]), "+v"(pv[4]), "+v"(pv[5]), "+v"(pv[6]), "+v"(pv[7])::"memory");
    float d = 0.f;
#pragma unroll
    for (int c = 0; c < 2; ++c)
#pragma unroll
      for (int e = 0; e < 4; ++e) {
        f32x2 t = (unp(pv[c * 4][e]) + unp(pv[c * 4 + 1][e])) + (unp(pv[c * 4 + 2][e]) + unp(pv[c * 4 + 3][e]));
        t = unp(pkv(t)) + bf[c][e];
        i16x2 hh = __builtin_bit_cast(i16x2, pkv(t));
        hh = __builtin_elementwise_max(hh, (i16x2){0, 0});
        const unsigned wq = w2[c][e >> 1][e & 1];
        d = __builtin_amdgcn_fdot2_f32_bf16(__builtin_bit_cast(bf16v2, hh), __builtin_bit_cast(bf16v2, wq), d, false);
      }
    d = sum16(d);
    d = rbf(rbf(d) + b2f);
    const float zz = rbf(wd + d);
    const __amdgpu_buffer_rsrc_t rO = __builtin_amdgcn_make_buffer_rsrc((void*)(u64)out, 0, kB * 2, 0x00020000);
    const __bf16 o = (__bf16)(1.f / (1.f + __expf(-zz)));
    if (lane < 4) __builtin_amdgcn_raw_buffer_store_b16(__builtin_bit_cast(unsigned short, o), rO, (G * 16 + p * 4 + lane) * 2, 0, SC1);
  }
}

extern "C" void kernel_launch(void* const* d_in, const int* in_sizes, int n_in, void* d_out, int out_size, void* d_ws, size_t ws_size,
                              hipStream_t stream) {
  (void)hipFuncSetAttribute((const void*)fused_kernel, hipFuncAttributeMaxDynamicSharedMemorySize, LDS_TOTAL);
  fused_kernel<<<256, 512, LDS_TOTAL, stream>>>(d_in[0], d_in[1], d_in[5], d_ws, d_in[6], d_in[7], d_in[2], d_in[3], d_in[4], d_in[8], d_out);
}
```
